```python
import torch

def _softmax(x, axis=-1):
    return torch.softmax(x, dim=axis)
def _gelu(x):
    return torch.nn.functional.gelu(x, approximate="tanh")
def _silu(x):
    return torch.nn.functional.silu(x)
def _sigmoid(x):
    return torch.sigmoid(x)
def _layernorm(x, axis=-1, eps=1e-5):
    m = x.mean(dim=axis, keepdim=True)
    v = x.var(dim=axis, keepdim=True, unbiased=False)
    return (x - m) / torch.sqrt(v + eps)
def _scatter_add(tgt, idx, src, axis=0):
    return tgt.clone().index_add_(axis, idx, src)
def _segment_sum(src, seg, n):
    out = torch.zeros((n,) + src.shape[1:], dtype=src.dtype)
    return out.index_add_(0, seg, src)
def _pad(x, pw):
    flat = tuple(v for pair in reversed(pw) for v in pair)
    return torch.nn.functional.pad(x, flat)

kernel_name = 'gated_mix__gqa_block_cached__glu_gated_glu__decode'

def setup_inputs():

    _w = (lambda a: (lambda t: t.to(torch.bfloat16) if t.is_floating_point() else t)(torch.as_tensor(a)))
    def _A_setup():
        B, Sq, D = 8, 1, 512
        Hq, Hkv, Sk, Dh = 32, 8, 8192, 128
        assert Hq % Hkv == 0, (Hq, Hkv)
        return {
            "x": _w((lambda *s: torch.randn(*s, dtype=torch.bfloat16) * 0.05)(B, Sq, D)),
            "k_cache": _w((lambda *s: torch.randn(*s, dtype=torch.bfloat16) * 6.4)(B, Hkv, Sk, Dh)),
            "v_cache": _w((lambda *s: torch.randn(*s, dtype=torch.bfloat16) * 0.4)(B, Hkv, Sk, Dh)),
            "wq": _w((lambda *s: torch.randn(*s, dtype=torch.bfloat16) * 6.4)(D, Hq * Dh) * (D ** -0.5)),
            "wo": _w((lambda *s: torch.randn(*s, dtype=torch.bfloat16) * 0.4)(Hq * Dh, D) * ((Hq * Dh) ** -0.5)),
        }
    _A_in = _A_setup()

    def _B_setup():
        B, S, D, F = 8, 1, 512, 1792
        return {
            "x": _w((lambda *s: torch.randn(*s, dtype=torch.bfloat16) * 0.05)(B, S, D)),
            "w_gate": _w((lambda *s: torch.randn(*s, dtype=torch.bfloat16) * 0.4)(D, F)),
            "w_up": _w((lambda *s: torch.randn(*s, dtype=torch.bfloat16) * 0.05)(D, F)),
            "w_down": _w((lambda *s: torch.randn(*s, dtype=torch.bfloat16) * 0.05)(F, D)),
        }
    _B_in = _B_setup()

    return {
        "A_x": _A_in["x"],
        "A_k_cache": _A_in["k_cache"],
        "A_v_cache": _A_in["v_cache"],
        "A_wq": _A_in["wq"],
        "A_wo": _A_in["wo"],
        "B_x": _B_in["x"],
        "B_w_gate": _B_in["w_gate"],
        "B_w_up": _B_in["w_up"],
        "B_w_down": _B_in["w_down"],
        "gate_w": _w((lambda *s: torch.randn(*s, dtype=torch.bfloat16) * 0.05)(512) * 16.0),
    }

def reference(A_x, A_k_cache, A_v_cache, A_wq, A_wo, B_x, B_w_gate, B_w_up, B_w_down, gate_w):
    def _A_ref(x, k_cache, v_cache, wq, wo):
        B, Sq, D = x.shape
        Hq, Dh = 32, 128
        Hkv = k_cache.shape[1]
        rep = Hq // Hkv
        q = (lambda x, *d: x.permute(*d))((x @ wq).reshape(B, Sq, Hq, Dh), 0, 2, 1, 3)
        k = (lambda x, n, axis: torch.repeat_interleave(x, n, dim=axis))(k_cache, rep, axis=1)
        v = (lambda x, n, axis: torch.repeat_interleave(x, n, dim=axis))(v_cache, rep, axis=1)
        scores = torch.einsum("bhqd,bhkd->bhqk", q, k) * (Dh ** -0.5)
        p = _softmax(scores, axis=-1)
        o = torch.einsum("bhqk,bhkd->bhqd", p, v)
        return (lambda x, *d: x.permute(*d))(o, 0, 2, 1, 3).reshape(B, Sq, Hq * Dh) @ wo

    def _B_ref(x, w_gate, w_up, w_down):
        g = torch.einsum("bsd,df->bsf", x, w_gate)
        u = torch.einsum("bsd,df->bsf", x, w_up)
        h = _sigmoid(g) * u
        return torch.einsum("bsf,fd->bsd", h, w_down)

    x = A_x
    g = _sigmoid((x * gate_w).sum(-1, keepdims=True))
    a = _A_ref(x, A_k_cache, A_v_cache, A_wq, A_wo)
    b = _B_ref(x, B_w_gate, B_w_up, B_w_down)
    return g * a + (1 - g) * b
```

```cpp
#include <hip/hip_runtime.h>
#include <hip/hip_bf16.h>
#include <cstdint>

#define GAS __attribute__((address_space(1)))
#define LAS __attribute__((address_space(3)))
typedef unsigned v4u __attribute__((ext_vector_type(4)));
typedef float f32x4 __attribute__((ext_vector_type(4)));
typedef short bf16x8 __attribute__((ext_vector_type(8)));
typedef unsigned short u16;

constexpr int NB = 8, DM = 512, HQ = 32, HKV = 8, SK = 8192, DH = 128, FF = 1792;
constexpr int NSPLIT = 4, KEYS_WG = SK / NSPLIT;
constexpr int NW = 8, TILES_WG = KEYS_WG / 32, TPW = TILES_WG / NW;
constexpr float C2 = 0.088388347648318440f * 1.4426950408889634f;

constexpr size_t WS_Q = 0;
constexpr size_t WS_H = WS_Q + NB * 4096 * 2;
constexpr size_t WS_O = WS_H + NB * FF * 2;
constexpr size_t WS_PO = WS_O + NB * 4096 * 2;
constexpr size_t WS_PML = WS_PO + NB * HQ * NSPLIT * DH * 4;

__device__ __forceinline__ f32x4 mfma(v4u a, v4u b, f32x4 c) {
  return __builtin_amdgcn_mfma_f32_16x16x32_bf16(__builtin_bit_cast(bf16x8, a), __builtin_bit_cast(bf16x8, b), c, 0, 0, 0);
}
__device__ __forceinline__ unsigned cvtpk(float lo, float hi) {
  typedef float f32x2_ __attribute__((ext_vector_type(2)));
  typedef __bf16 bf16x2_ __attribute__((ext_vector_type(2)));
  return __builtin_bit_cast(unsigned, __builtin_convertvector((f32x2_{lo, hi}), bf16x2_));
}
__device__ __forceinline__ float bf2f(u16 b) { return __builtin_bit_cast(float, (unsigned)b << 16); }
__device__ __forceinline__ float rbf(float x) { return __builtin_bit_cast(float, cvtpk(x, 0.f) << 16); }
__device__ __forceinline__ float xrow_max(float x) {
  auto s = __builtin_amdgcn_permlane16_swap(__float_as_uint(x), __float_as_uint(x), false, false);
  x = fmaxf(__uint_as_float(s[0]), __uint_as_float(s[1]));
  auto t = __builtin_amdgcn_permlane32_swap(__float_as_uint(x), __float_as_uint(x), false, false);
  return fmaxf(__uint_as_float(t[0]), __uint_as_float(t[1]));
}
__device__ __forceinline__ float xrow_sum(float x) {
  auto s = __builtin_amdgcn_permlane16_swap(__float_as_uint(x), __float_as_uint(x), false, false);
  x = __uint_as_float(s[0]) + __uint_as_float(s[1]);
  auto t = __builtin_amdgcn_permlane32_swap(__float_as_uint(x), __float_as_uint(x), false, false);
  return __uint_as_float(t[0]) + __uint_as_float(t[1]);
}
__device__ __forceinline__ v4u ldBfrag(const u16* W, int ld, int k0, int c0, int lane) {
  const u16* p = W + (size_t)(k0 + 8 * (lane >> 4)) * ld + c0 + (lane & 15);
  v4u f;
#pragma unroll
  for (int u = 0; u < 4; ++u) f[u] = (unsigned)p[(2 * u) * ld] | ((unsigned)p[(2 * u + 1) * ld] << 16);
  return f;
}
__device__ __forceinline__ v4u ldAfrag(const u16* X, int ld, int k0, int lane) {
  return *(const v4u*)(X + (size_t)(lane & 7) * ld + k0 + 8 * (lane >> 4));
}

__global__ void __launch_bounds__(512) k_prep(const u16* x, const u16* wq, const u16* wg, const u16* wu, unsigned char* ws) {
  __shared__ float red[2][8][128];
  const int lane = threadIdx.x & 63, w = threadIdx.x >> 6, bid = blockIdx.x;
  const bool isq = bid < 256;
  f32x4 a0 = {0, 0, 0, 0}, a1 = {0, 0, 0, 0};
#pragma unroll
  for (int s = 0; s < 2; ++s) {
    const int k0 = 64 * w + 32 * s;
    const v4u xa = ldAfrag(x, DM, k0, lane);
    if (isq) a0 = mfma(xa, ldBfrag(wq, 4096, k0, 16 * bid, lane), a0);
    else { a0 = mfma(xa, ldBfrag(wg, FF, k0, 16 * (bid - 256), lane), a0); a1 = mfma(xa, ldBfrag(wu, FF, k0, 16 * (bid - 256), lane), a1); }
  }
  if (lane < 32) {
#pragma unroll
    for (int g = 0; g < 4; ++g) { red[0][w][(4 * (lane >> 4) + g) * 16 + (lane & 15)] = a0[g]; red[1][w][(4 * (lane >> 4) + g) * 16 + (lane & 15)] = a1[g]; }
  }
  __syncthreads();
  if (threadIdx.x < 128) {
    float s0 = 0.f, s1 = 0.f;
#pragma unroll
    for (int ww = 0; ww < 8; ++ww) { s0 += red[0][ww][threadIdx.x]; s1 += red[1][ww][threadIdx.x]; }
    const int b = threadIdx.x >> 4, c = threadIdx.x & 15;
    if (isq) ((u16*)(ws + WS_Q))[b * 4096 + 16 * bid + c] = (u16)cvtpk(s0, 0.f);
    else { const float g = rbf(s0), u = rbf(s1); const float sg = rbf(1.f / (1.f + __expf(-g)));
      ((u16*)(ws + WS_H))[b * FF + 16 * (bid - 256) + c] = (u16)cvtpk(sg * u, 0.f); }
  }
}

__global__ void __launch_bounds__(512, 2) k_attn(const u16* __restrict__ Kc, const u16* __restrict__ Vc, unsigned char* ws) {
  __shared__ __attribute__((aligned(16))) float lo[NW][4][DH];
  __shared__ float lml[NW][4][2];
  const int lane = threadIdx.x & 63, w = __builtin_amdgcn_readfirstlane(threadIdx.x >> 6);
  const int bid = blockIdx.x, kvh = bid & 7, j = bid >> 3, b = j >> 2, sp = j & 3;
  const int r = lane & 15, h = lane >> 4;
  const size_t base = ((size_t)(b * HKV + kvh) * SK + (size_t)sp * KEYS_WG) * DH;
  const __amdgpu_buffer_rsrc_t rk = __builtin_amdgcn_make_buffer_rsrc((void*)(Kc + base), 0, KEYS_WG * DH * 2, 0x00020000);
  const __amdgpu_buffer_rsrc_t rv = __builtin_amdgcn_make_buffer_rsrc((void*)(Vc + base), 0, KEYS_WG * DH * 2, 0x00020000);
  const int kl = (4 * (r & 3) + (r >> 2)) * 256 + h * 16, vl = lane * 16;
  v4u kb[2][8], vb[2][8];
#define ISSUE_K(d, t) do { const int so_ = (w + (t) * NW) * 8192; _Pragma("unroll") for (int i = 0; i < 8; ++i) kb[d][i] = __builtin_amdgcn_raw_buffer_load_b128(rk, kl + (i >> 2) * 4096 + (i & 3) * 64, so_, 2); __builtin_amdgcn_sched_barrier(0); } while (0)
#define ISSUE_V(d, t) do { const int so_ = (w + (t) * NW) * 8192; _Pragma("unroll") for (int i = 0; i < 8; ++i) vb[d][i] = __builtin_amdgcn_raw_buffer_load_b128(rv, vl + i * 1024, so_, 2); __builtin_amdgcn_sched_barrier(0); } while (0)
  v4u qf[4];
  {
    const u16* q = (const u16*)(ws + WS_Q) + b * 4096 + (kvh * 4 + (r & 3)) * DH + 8 * h;
#pragma unroll
    for (int i = 0; i < 4; ++i) qf[i] = *(const v4u*)(q + 32 * i);
  }
  __builtin_amdgcn_sched_barrier(0);
  ISSUE_K(0, 0); ISSUE_V(0, 0); ISSUE_K(1, 1); ISSUE_V(1, 1);
  f32x4 acc[8];
#pragma unroll
  for (int d = 0; d < 8; ++d) acc[d] = f32x4{0, 0, 0, 0};
  float m = -INFINITY, l = 0.f;
#define STEP(d, tn, REFILL) do { \
    f32x4 s0 = {0, 0, 0, 0}, s1 = {0, 0, 0, 0}; \
    _Pragma("unroll") for (int i = 0; i < 4; ++i) { s0 = mfma(kb[d][i], qf[i], s0); s1 = mfma(kb[d][4 + i], qf[i], s1); } \
    asm volatile("" : "+v"(s0), "+v"(s1)); \
    __builtin_amdgcn_sched_barrier(0); \
    if (REFILL) ISSUE_K(d, tn); \
    float tm = fmaxf(fmaxf(fmaxf(s0[0], s0[1]), fmaxf(s0[2], s0[3])), fmaxf(fmaxf(s1[0], s1[1]), fmaxf(s1[2], s1[3]))); \
    tm = xrow_max(tm) * C2; \
    const float mn = fmaxf(m, tm), alpha = __builtin_amdgcn_exp2f(m - mn); m = mn; \
    float p[8]; \
    _Pragma("unroll") for (int i = 0; i < 4; ++i) { p[i] = __builtin_amdgcn_exp2f(fmaf(s0[i], C2, -mn)); p[4 + i] = __builtin_amdgcn_exp2f(fmaf(s1[i], C2, -mn)); } \
    l = fmaf(l, alpha, ((p[0] + p[1]) + (p[2] + p[3])) + ((p[4] + p[5]) + (p[6] + p[7]))); \
    const v4u pf = {cvtpk(p[0], p[1]), cvtpk(p[2], p[3]), cvtpk(p[4], p[5]), cvtpk(p[6], p[7])}; \
    _Pragma("unroll") for (int dd = 0; dd < 8; ++dd) { \
      v4u a; _Pragma("unroll") for (int u = 0; u < 4; ++u) a[u] = __builtin_amdgcn_perm(vb[d][2 * u + 1][dd >> 1], vb[d][2 * u][dd >> 1], (dd & 1) ? 0x07060302u : 0x05040100u); \
      acc[dd] = mfma(a, pf, acc[dd] * alpha); } \
    asm volatile("" : "+v"(acc[0]), "+v"(acc[1]), "+v"(acc[2]), "+v"(acc[3]), "+v"(acc[4]), "+v"(acc[5]), "+v"(acc[6]), "+v"(acc[7])); \
    __builtin_amdgcn_sched_barrier(0); \
    if (REFILL) ISSUE_V(d, tn); \
  } while (0)
#pragma unroll 1
  for (int it = 0; it < TPW - 2; it += 2) { STEP(0, it + 2, true); STEP(1, it + 3, true); }
  STEP(0, 0, false); STEP(1, 0, false);
  l = xrow_sum(l);
  if (r < 4) {
#pragma unroll
    for (int g = 0; g < 4; ++g) {
      float* dst = &lo[w][r][8 * (4 * h + g)];
      *(f32x4*)dst = f32x4{acc[0][g], acc[1][g], acc[2][g], acc[3][g]};
      *(f32x4*)(dst + 4) = f32x4{acc[4][g], acc[5][g], acc[6][g], acc[7][g]};
    }
    if (h == 0) { lml[w][r][0] = m; lml[w][r][1] = l; }
  }
  __syncthreads();
  {
    const int hd = threadIdx.x >> 7, dm = threadIdx.x & 127;
    float M = -INFINITY;
#pragma unroll
    for (int ww = 0; ww < NW; ++ww) M = fmaxf(M, lml[ww][hd][0]);
    float L = 0.f, o = 0.f;
#pragma unroll
    for (int ww = 0; ww < NW; ++ww) { const float wt = __builtin_amdgcn_exp2f(lml[ww][hd][0] - M); L = fmaf(wt, lml[ww][hd][1], L); o = fmaf(wt, lo[ww][hd][dm], o); }
    const int slot = (b * HQ + kvh * 4 + hd) * NSPLIT + sp;
    ((float*)(ws + WS_PO))[slot * DH + dm] = o;
    if (dm == 0) { ((float*)(ws + WS_PML))[slot * 2] = M; ((float*)(ws + WS_PML))[slot * 2 + 1] = L; }
  }
}

__global__ void __launch_bounds__(128) k_comb(unsigned char* ws) {
  const int row = blockIdx.x, dm = threadIdx.x;
  const float* pml = (const float*)(ws + WS_PML) + row * NSPLIT * 2;
  const float* po = (const float*)(ws + WS_PO) + row * NSPLIT * DH;
  float M = -INFINITY;
#pragma unroll
  for (int s = 0; s < NSPLIT; ++s) M = fmaxf(M, pml[2 * s]);
  float L = 0.f, o = 0.f;
#pragma unroll
  for (int s = 0; s < NSPLIT; ++s) { const float wt = __builtin_amdgcn_exp2f(pml[2 * s] - M); L = fmaf(wt, pml[2 * s + 1], L); o = fmaf(wt, po[s * DH + dm], o); }
  ((u16*)(ws + WS_O))[row * DH + dm] = (u16)cvtpk(o / L, 0.f);
}

__global__ void __launch_bounds__(1024) k_final(const u16* x, const u16* wo, const u16* wd, const u16* gw, const unsigned char* ws, u16* out) {
  __shared__ float red[2][16][128];
  __shared__ float gs[8];
  const int lane = threadIdx.x & 63, w = threadIdx.x >> 6, c0 = 16 * blockIdx.x;
  f32x4 a0 = {0, 0, 0, 0}, a1 = {0, 0, 0, 0};
  const u16* o = (const u16*)(ws + WS_O); const u16* hh = (const u16*)(ws + WS_H);
#pragma unroll
  for (int s = 0; s < 8; ++s) { const int k0 = 256 * w + 32 * s; a0 = mfma(ldAfrag(o, 4096, k0, lane), ldBfrag(wo, DM, k0, c0, lane), a0); }
  for (int s = w; s < FF / 32; s += 16) { const int k0 = 32 * s; a1 = mfma(ldAfrag(hh, FF, k0, lane), ldBfrag(wd, DM, k0, c0, lane), a1); }
  if (w < 8) {
    float s = 0.f;
#pragma unroll
    for (int i = 0; i < 8; ++i) s += rbf(bf2f(x[w * DM + lane * 8 + i]) * bf2f(gw[lane * 8 + i]));
#pragma unroll
    for (int mm = 32; mm >= 1; mm >>= 1) s += __shfl_xor(s, mm);
    if (lane == 0) gs[w] = rbf(1.f / (1.f + __expf(-rbf(s))));
  }
  if (lane < 32) {
#pragma unroll
    for (int g = 0; g < 4; ++g) { red[0][w][(4 * (lane >> 4) + g) * 16 + (lane & 15)] = a0[g]; red[1][w][(4 * (lane >> 4) + g) * 16 + (lane & 15)] = a1[g]; }
  }
  __syncthreads();
  if (threadIdx.x < 128) {
    float s0 = 0.f, s1 = 0.f;
#pragma unroll
    for (int ww = 0; ww < 16; ++ww) { s0 += red[0][ww][threadIdx.x]; s1 += red[1][ww][threadIdx.x]; }
    const int b = threadIdx.x >> 4, c = threadIdx.x & 15;
    const float g = gs[b];
    out[b * DM + c0 + c] = (u16)cvtpk(rbf(g * rbf(s0)) + rbf(rbf(1.f - g) * rbf(s1)), 0.f);
  }
}

extern "C" void kernel_launch(void* const* d_in, const int* in_sizes, int n_in, void* d_out, int out_size, void* d_ws, size_t ws_size, hipStream_t stream) {
  const u16* x = (const u16*)d_in[0]; const u16* kc = (const u16*)d_in[1]; const u16* vc = (const u16*)d_in[2];
  const u16* wq = (const u16*)d_in[3]; const u16* wo = (const u16*)d_in[4];
  const u16* wg = (const u16*)d_in[6]; const u16* wu = (const u16*)d_in[7]; const u16* wd = (const u16*)d_in[8]; const u16* gw = (const u16*)d_in[9];
  unsigned char* ws = (unsigned char*)d_ws;
  k_prep<<<256 + FF / 16, 512, 0, stream>>>(x, wq, wg, wu, ws);
  k_attn<<<256, 512, 0, stream>>>(kc, vc, ws);
  k_comb<<<NB * HQ, 128, 0, stream>>>(ws);
  k_final<<<DM / 16, 1024, 0, stream>>>(x, wo, wd, gw, ws, (u16*)d_out);
}
```

```cpp
#include <hip/hip_runtime.h>
#include <hip/hip_bf16.h>
#include <cstdint>

#define LAS __attribute__((address_space(3)))
typedef unsigned v4u __attribute__((ext_vector_type(4)));
typedef unsigned v2u __attribute__((ext_vector_type(2)));
typedef float f32x4 __attribute__((ext_vector_type(4)));
typedef float f32x2 __attribute__((ext_vector_type(2)));
typedef short bf16x8 __attribute__((ext_vector_type(8)));
typedef unsigned short u16;

#define ILV 1

constexpr int NB = 8, DM = 512, HQ = 32, HKV = 8, SK = 8192, DH = 128, FF = 1792;
constexpr int NSPLIT = 4, NW = 8, TILES_WG = SK / NSPLIT / 32;
constexpr float C2 = 0.088388347648318440f * 1.4426950408889634f;
constexpr int AUX_NT = 2, AUX_SC1 = 16, RS3 = 0x00020000;
constexpr unsigned SPIN_CAP = 1u << 18;

constexpr int WS_E = 0;
constexpr int WS_QG = 4096;
constexpr int WS_HG = WS_QG + 8 * 8 * 256 * 8;
constexpr int WS_PO = WS_HG + 8 * FF * 8;
constexpr int WS_ML = WS_PO + 32 * 8 * 4 * 128 * 8;
constexpr int WS_P3 = WS_ML + 32 * 8 * 4 * 2 * 8;
constexpr int WS_END = WS_P3 + 256 * 64 * 16 * 8;
constexpr int WS_ST = WS_END;
#define STAMP(i) do { } while (0)

constexpr int L_RED = 0;
constexpr int L_QS = L_RED + 8192;
constexpr int L_WOF = L_QS + 1024;
constexpr int L_DB = L_WOF + 16384;
constexpr int L_LO = L_DB + 8192;
constexpr int L_ML = L_LO + 16384;
constexpr int L_MISC = L_ML + 256;
constexpr int L_END = L_MISC + 64;

__device__ __forceinline__ f32x4 mfma(v4u a, v4u b, f32x4 c) {
  return __builtin_amdgcn_mfma_f32_16x16x32_bf16(__builtin_bit_cast(bf16x8, a), __builtin_bit_cast(bf16x8, b), c, 0, 0, 0);
}
__device__ __forceinline__ unsigned cvtpk(float lo, float hi) {
  typedef __bf16 bf16x2_ __attribute__((ext_vector_type(2)));
  return __builtin_bit_cast(unsigned, __builtin_convertvector((f32x2{lo, hi}), bf16x2_));
}
__device__ __forceinline__ float u2f(unsigned u) { return __builtin_bit_cast(float, u); }
__device__ __forceinline__ unsigned f2u(float f) { return __builtin_bit_cast(unsigned, f); }
__device__ __forceinline__ float rbf(float x) { return u2f(cvtpk(x, 0.f) << 16); }
__device__ __forceinline__ float xrow_max(float x) {
  auto s = __builtin_amdgcn_permlane16_swap(f2u(x), f2u(x), false, false);
  x = fmaxf(u2f(s[0]), u2f(s[1]));
  auto t = __builtin_amdgcn_permlane32_swap(f2u(x), f2u(x), false, false);
  return fmaxf(u2f(t[0]), u2f(t[1]));
}
__device__ __forceinline__ float xrow_sum(float x) {
  auto s = __builtin_amdgcn_permlane16_swap(f2u(x), f2u(x), false, false);
  x = u2f(s[0]) + u2f(s[1]);
  auto t = __builtin_amdgcn_permlane32_swap(f2u(x), f2u(x), false, false);
  return u2f(t[0]) + u2f(t[1]);
}
__device__ __forceinline__ float ror8(float x) { return u2f((unsigned)__builtin_amdgcn_mov_dpp((int)f2u(x), 0x128, 0xf, 0xf, true)); }
__device__ __forceinline__ float sigmoidf(float x) { return 1.f / (1.f + __expf(-x)); }
__device__ __forceinline__ v4u tr_frag(const v4u (&v)[8], int d) {
  v4u a;
#pragma unroll
  for (int u = 0; u < 4; ++u) a[u] = __builtin_amdgcn_perm(v[2 * u + 1][d >> 1], v[2 * u][d >> 1], (d & 1) ? 0x07060302u : 0x05040100u);
  return a;
}
#define SB() do { asm volatile("" ::: "memory"); __builtin_amdgcn_sched_barrier(0); } while (0)

__global__ void __launch_bounds__(512, 2) k_fused(const u16* __restrict__ x, const u16* Kc, const u16* Vc, const u16* __restrict__ wq,
                                                  const u16* __restrict__ wo, const u16* __restrict__ wg, const u16* __restrict__ wu, const u16* __restrict__ wd,
                                                  const u16* __restrict__ gw, unsigned char* ws, unsigned* out) {
  __shared__ __attribute__((aligned(16))) unsigned char smem[L_END];
  LAS unsigned char* L = (LAS unsigned char*)smem;
  volatile LAS unsigned* misc = (volatile LAS unsigned*)(L + L_MISC);
  const int tid = threadIdx.x, lane = tid & 63, w = __builtin_amdgcn_readfirstlane(tid >> 6);
  const int bid = blockIdx.x, kvh = bid & 7, j = bid >> 3, b = j >> 2, sp = j & 3;
  const int r = lane & 15, h = lane >> 4;
  STAMP(0);
  const __amdgpu_buffer_rsrc_t rw = __builtin_amdgcn_make_buffer_rsrc((void*)ws, 0, WS_END, RS3);
  v4u xa[2], bm[2];
  unsigned epoch, q16[16], g16[16];
  {
    const int k0 = 64 * w + 8 * h;
    const __amdgpu_buffer_rsrc_t rq = __builtin_amdgcn_make_buffer_rsrc((void*)wq, 0, DM * 4096 * 2, RS3);
    const int qoff = k0 * 8192 + (kvh * 512 + 16 * j + r) * 2;
    const u16* pg = (r < 8 ? wg : wu) + (size_t)k0 * FF + 7 * (kvh * 32 + j) + min(r & 7, 6);
#pragma unroll
    for (int i = 0; i < 16; ++i) q16[i] = __builtin_amdgcn_raw_buffer_load_b16(rq, qoff, (32 * (i >> 3) + (i & 7)) * 8192, AUX_NT);
#pragma unroll
    for (int s = 0; s < 2; ++s) xa[s] = *(const v4u*)(x + (lane & 7) * DM + k0 + 32 * s);
    epoch = __builtin_amdgcn_raw_buffer_load_b32(rw, WS_E, 0, AUX_SC1);
    SB();
#pragma unroll
    for (int s = 0; s < 2; ++s) bm[s] = *(const v4u*)(gw + k0 + 32 * s);
#pragma unroll
    for (int i = 0; i < 16; ++i) g16[i] = __builtin_nontemporal_load(pg + (32 * (i >> 3) + (i & 7)) * FF);
  }
  SB();
  const size_t base = ILV ? (size_t)(b * HKV + kvh) * SK * DH : ((size_t)(b * HKV + kvh) * SK + (size_t)sp * (SK / NSPLIT)) * DH;
  constexpr int KV_BYTES = ILV ? SK * DH * 2 : SK * DH * 2 / NSPLIT;
  const int kl = (4 * (r & 3) + (r >> 2)) * 256 + h * 16, vl = lane * 16;
  v4u kb[2][8], vb[2][8];
#define TOFF(t) (ILV ? ((t) * NSPLIT + sp) * 8192 : (t) * 8192)
#define ISSUE_K(d, t) do { const int t_ = (t); const __amdgpu_buffer_rsrc_t rs_ = __builtin_amdgcn_make_buffer_rsrc((void*)(Kc + base), 0, t_ < TILES_WG ? KV_BYTES : 0, RS3); const int so_ = TOFF(t_ & (TILES_WG - 1)); \
    _Pragma("unroll") for (int i = 0; i < 8; ++i) kb[d][i] = __builtin_amdgcn_raw_buffer_load_b128(rs_, kl + (i >> 2) * 4096 + (i & 3) * 64, so_, AUX_NT); SB(); } while (0)
#define ISSUE_V(d, t) do { const int t_ = (t); const __amdgpu_buffer_rsrc_t rs_ = __builtin_amdgcn_make_buffer_rsrc((void*)(Vc + base), 0, t_ < TILES_WG ? KV_BYTES : 0, RS3); const int so_ = TOFF(t_ & (TILES_WG - 1)); \
    _Pragma("unroll") for (int i = 0; i < 8; ++i) vb[d][i] = __builtin_amdgcn_raw_buffer_load_b128(rs_, vl + i * 1024, so_, AUX_NT); SB(); } while (0)
  const int unit = kvh * 8 + (j >> 2), cg = j & 3;
  int t0 = w >= 2 ? w - 2 : TILES_WG, t1 = w + 4;
  ISSUE_K(0, t0);
  {
    f32x4 a0 = {0, 0, 0, 0}, a1 = {0, 0, 0, 0}, a2 = {0, 0, 0, 0};
    v4u bq[2], bg[2];
#pragma unroll
    for (int i = 0; i < 8; ++i) { bq[i >> 2][i & 3] = q16[2 * i] | (q16[2 * i + 1] << 16); bg[i >> 2][i & 3] = g16[2 * i] | (g16[2 * i + 1] << 16); }
#pragma unroll
    for (int s = 0; s < 2; ++s) { a0 = mfma(xa[s], bq[s], a0); a1 = mfma(xa[s], bg[s], a1); a2 = mfma(xa[s], bm[s], a2); }
    if (lane < 32) {
      LAS float* red = (LAS float*)(L + L_RED) + w * 256;
#pragma unroll
      for (int g = 0; g < 4; ++g) { red[(4 * h + g) * 16 + r] = a0[g]; red[128 + (4 * h + g) * 16 + r] = (r == 7) ? a2[g] : a1[g]; }
    }
    if (tid == 0) { misc[0] = 12u; misc[1] = 0u; misc[2] = epoch + 1u; }
  }
  __syncthreads();
  STAMP(1);
  const unsigned tag = __builtin_amdgcn_readfirstlane(misc[2]);
  if (w == 0) {
    f32x2 s = {0.f, 0.f};
#pragma unroll
    for (int ww = 0; ww < NW; ++ww) s += *(const LAS f32x2*)(L + L_RED + ww * 1024 + lane * 8);
    __builtin_amdgcn_raw_buffer_store_b64(v2u{cvtpk(s.x, s.y), tag}, rw, WS_QG + ((kvh * 8 + (lane >> 3)) * 256 + 8 * j + (lane & 7)) * 8, 0, AUX_SC1);
    STAMP(2);
    const int qo = WS_QG + (kvh * 8 + b) * 2048 + lane * 32;
    v4u g0, g1;
    for (unsigned spins = 0; spins < SPIN_CAP; ++spins) {
      asm volatile("" ::: "memory");
      g0 = __builtin_amdgcn_raw_buffer_load_b128(rw, qo, 0, AUX_SC1);
      g1 = __builtin_amdgcn_raw_buffer_load_b128(rw, qo + 16, 0, AUX_SC1);
      if (__all(g0.y == tag && g0.w == tag && g1.y == tag && g1.w == tag)) break;
    }
    *(LAS v4u*)(L + L_QS + lane * 16) = v4u{g0.x, g0.z, g1.x, g1.z};
    asm volatile("s_waitcnt lgkmcnt(0)" ::: "memory");
    misc[1] = 1u;
    STAMP(3);
  } else if (w == 1) {
    v4u wr[2][8], dr[8];
    {
      const u16* po = wo + (size_t)(unit * 64 + 8 * h) * DM + cg * 128 + 8 * r;
#pragma unroll
      for (int s = 0; s < 2; ++s)
#pragma unroll
        for (int i = 0; i < 8; ++i) wr[s][i] = __builtin_nontemporal_load((const v4u*)(po + (32 * s + i) * DM));
      const u16* pd = wd + (size_t)(unit * 28 + 8 * h) * DM + cg * 128 + 8 * r;
#pragma unroll
      for (int i = 0; i < 8; ++i) { const bool ok = i < 4 || h < 3; dr[i] = __builtin_nontemporal_load((const v4u*)(pd + (ok ? i : 0) * DM)); }
      SB();
    }
    {
      const int bb = lane >> 3, c = lane & 7;
      float sg = 0.f, su = 0.f;
#pragma unroll
      for (int ww = 0; ww < NW; ++ww) { const LAS float* red = (const LAS float*)(L + L_RED) + ww * 256 + 128 + bb * 16 + c; sg += red[0]; su += red[8]; }
      const float sig = rbf(sigmoidf(rbf(sg)));
      if (c < 7) __builtin_amdgcn_raw_buffer_store_b64(v2u{f2u(rbf(sig * rbf(su))), tag}, rw, WS_HG + (bb * FF + 7 * (kvh * 32 + j) + c) * 8, 0, AUX_SC1);
      else misc[8 + bb] = f2u(sig);
    }
#pragma unroll
    for (int s = 0; s < 2; ++s)
#pragma unroll
      for (int d = 0; d < 8; ++d) *(LAS v4u*)(L + L_WOF + (s * 8 + d) * 1024 + lane * 16) = tr_frag(wr[s], d);
    const int ho = WS_HG + ((r & 7) * FF + unit * 28 + 8 * h) * 8;
    v4u hg[4];
    for (unsigned spins = 0; spins < SPIN_CAP; ++spins) {
      asm volatile("" ::: "memory");
      bool ok = true;
#pragma unroll
      for (int u = 0; u < 4; ++u) { hg[u] = __builtin_amdgcn_raw_buffer_load_b128(rw, ho + ((u < 2 || h < 3) ? u : 0) * 16, 0, AUX_SC1); ok = ok && hg[u].y == tag && hg[u].w == tag; }
      if (__all(ok)) break;
    }
    v4u hb;
#pragma unroll
    for (int u = 0; u < 4; ++u) hb[u] = ((u < 2 || h < 3) && r < 8) ? cvtpk(u2f(hg[u].x), u2f(hg[u].z)) : 0u;
#pragma unroll
    for (int i = 4; i < 8; ++i) dr[i] = h < 3 ? dr[i] : v4u{0u, 0u, 0u, 0u};
    asm volatile("s_waitcnt lgkmcnt(0)" ::: "memory");
    const float omg = rbf(1.f - u2f(misc[8 + (r & 7)]));
#pragma unroll
    for (int d = 0; d < 8; ++d) *(LAS f32x4*)(L + L_DB + d * 1024 + lane * 16) = mfma(tr_frag(dr, d), hb, f32x4{0, 0, 0, 0}) * omg;
    STAMP(3);
  }
#define NEXT_TILE() __builtin_amdgcn_readfirstlane(lane == 0 ? __hip_atomic_fetch_add((LAS unsigned*)(L + L_MISC), 1u, __ATOMIC_RELAXED, __HIP_MEMORY_SCOPE_WORKGROUP) : 0u)
#define THROTTLE() do { if (__builtin_amdgcn_readfirstlane(misc[1]) == 0u) asm volatile("s_waitcnt vmcnt(0)" ::: "memory"); } while (0)
  if (w < 2) { t0 = NEXT_TILE(); t1 = NEXT_TILE(); ISSUE_K(0, t0); }
  THROTTLE(); ISSUE_V(0, t0);
  THROTTLE(); ISSUE_K(1, t1);
  THROTTLE(); ISSUE_V(1, t1);
  while (__builtin_amdgcn_readfirstlane(misc[1]) == 0u) __builtin_amdgcn_s_sleep(1);
  asm volatile("" ::: "memory");
  v4u qf[4];
#pragma unroll
  for (int i = 0; i < 4; ++i) qf[i] = *(const LAS v4u*)(L + L_QS + (r & 3) * 256 + i * 64 + h * 16);
  STAMP(4);
  f32x4 acc[8];
#pragma unroll
  for (int d = 0; d < 8; ++d) acc[d] = f32x4{0, 0, 0, 0};
  float m = -INFINITY, l = 0.f;
#define STEP(d, tcur) do { \
    const int tn_ = NEXT_TILE(); \
    f32x4 s0 = {0, 0, 0, 0}, s1 = {0, 0, 0, 0}; \
    _Pragma("unroll") for (int i = 0; i < 4; ++i) { s0 = mfma(kb[d][i], qf[i], s0); s1 = mfma(kb[d][4 + i], qf[i], s1); } \
    asm volatile("" : "+v"(s0), "+v"(s1)); \
    SB(); \
    ISSUE_K(d, tn_); \
    float tm = fmaxf(fmaxf(fmaxf(s0[0], s0[1]), fmaxf(s0[2], s0[3])), fmaxf(fmaxf(s1[0], s1[1]), fmaxf(s1[2], s1[3]))); \
    tm = xrow_max(tm) * C2; \
    const float mn = fmaxf(m, tm), alpha = __builtin_amdgcn_exp2f(m - mn); m = mn; \
    float p[8]; \
    _Pragma("unroll") for (int i = 0; i < 4; ++i) { p[i] = __builtin_amdgcn_exp2f(fmaf(s0[i], C2, -mn)); p[4 + i] = __builtin_amdgcn_exp2f(fmaf(s1[i], C2, -mn)); } \
    l = fmaf(l, alpha, ((p[0] + p[1]) + (p[2] + p[3])) + ((p[4] + p[5]) + (p[6] + p[7]))); \
    const v4u pf = {cvtpk(p[0], p[1]), cvtpk(p[2], p[3]), cvtpk(p[4], p[5]), cvtpk(p[6], p[7])}; \
    _Pragma("unroll") for (int dd = 0; dd < 8; ++dd) acc[dd] = mfma(tr_frag(vb[d], dd), pf, acc[dd] * alpha); \
    asm volatile("" : "+v"(acc[0]), "+v"(acc[1]), "+v"(acc[2]), "+v"(acc[3]), "+v"(acc[4]), "+v"(acc[5]), "+v"(acc[6]), "+v"(acc[7])); \
    SB(); \
    ISSUE_V(d, tn_); \
    tcur = tn_; \
  } while (0)
#pragma unroll 1
  while (true) {
    if (t0 >= TILES_WG) break;
    STEP(0, t0);
    if (t1 >= TILES_WG) break;
    STEP(1, t1);
  }
  STAMP(5);
  l = xrow_sum(l);
  if (r < 4) {
#pragma unroll
    for (int g = 0; g < 4; ++g) {
      LAS float* dst = (LAS float*)(L + L_LO) + (w * 4 + r) * DH + 8 * (4 * h + g);
      *(LAS f32x4*)dst = f32x4{acc[0][g], acc[1][g], acc[2][g], acc[3][g]};
      *(LAS f32x4*)(dst + 4) = f32x4{acc[4][g], acc[5][g], acc[6][g], acc[7][g]};
    }
    if (h == 0) *(LAS f32x2*)(L + L_ML + (w * 4 + r) * 8) = f32x2{m, l};
  }
  __syncthreads();
  {
    const int hd = tid >> 7, dm = tid & 127;
    f32x2 ml[NW];
    float M = -INFINITY;
#pragma unroll
    for (int ww = 0; ww < NW; ++ww) { ml[ww] = *(const LAS f32x2*)(L + L_ML + (ww * 4 + hd) * 8); M = fmaxf(M, ml[ww].x); }
    float Ls = 0.f, o = 0.f;
#pragma unroll
    for (int ww = 0; ww < NW; ++ww) { const float wt = __builtin_amdgcn_exp2f(ml[ww].x - M); Ls = fmaf(wt, ml[ww].y, Ls); o = fmaf(wt, ((const LAS float*)(L + L_LO))[(ww * 4 + hd) * DH + dm], o); }
    const int slot = ((kvh * 4 + hd) * 8 + b) * NSPLIT + sp;
    __builtin_amdgcn_raw_buffer_store_b64(v2u{f2u(o), tag}, rw, WS_PO + (slot * DH + dm) * 8, 0, AUX_SC1);
    if (dm == 0) __builtin_amdgcn_raw_buffer_store_b128(v4u{f2u(M), tag, f2u(Ls), tag}, rw, WS_ML + slot * 16, 0, AUX_SC1);
  }
  STAMP(6);
  if (w == 0) {
    const int head = unit >> 1, bb = r & 7, sh = r >> 3;
    v4u wf[16]; f32x4 dacc[8];
#pragma unroll
    for (int i = 0; i < 16; ++i) wf[i] = *(const LAS v4u*)(L + L_WOF + i * 1024 + lane * 16);
#pragma unroll
    for (int d = 0; d < 8; ++d) dacc[d] = *(const LAS f32x4*)(L + L_DB + d * 1024 + lane * 16);
    const float gmix = u2f(misc[8 + bb]);
    const int mo = WS_ML + (head * 8 + bb) * NSPLIT * 16;
    const int oo = WS_PO + (((head * 8 + bb) * NSPLIT + 2 * sh) * DH + (unit & 1) * 64 + 8 * h) * 8;
    v4u mlg[4], og[2][2][4];
    for (unsigned spins = 0; spins < SPIN_CAP; ++spins) {
      asm volatile("" ::: "memory");
      bool ok = true;
#pragma unroll
      for (int s = 0; s < 4; ++s) mlg[s] = __builtin_amdgcn_raw_buffer_load_b128(rw, mo + s * 16, 0, AUX_SC1);
#pragma unroll
      for (int ks = 0; ks < 2; ++ks)
#pragma unroll
        for (int s = 0; s < 2; ++s)
#pragma unroll
          for (int u = 0; u < 4; ++u) og[ks][s][u] = __builtin_amdgcn_raw_buffer_load_b128(rw, oo + (s * DH + ks * 32) * 8 + u * 16, 0, AUX_SC1);
#pragma unroll
      for (int s = 0; s < 4; ++s) ok = ok && mlg[s].y == tag && mlg[s].w == tag;
#pragma unroll
      for (int ks = 0; ks < 2; ++ks)
#pragma unroll
        for (int s = 0; s < 2; ++s)
#pragma unroll
          for (int u = 0; u < 4; ++u) ok = ok && og[ks][s][u].y == tag && og[ks][s][u].w == tag;
      if (__all(ok)) break;
    }
    const float M = fmaxf(fmaxf(u2f(mlg[0].x), u2f(mlg[1].x)), fmaxf(u2f(mlg[2].x), u2f(mlg[3].x)));
    float wt[4], Ls = 0.f;
#pragma unroll
    for (int s = 0; s < 4; ++s) { wt[s] = __builtin_amdgcn_exp2f(u2f(mlg[s].x) - M); Ls = fmaf(wt[s], u2f(mlg[s].z), Ls); }
    const float sc = gmix / Ls;
    const float w0 = (sh ? wt[2] : wt[0]) * sc, w1 = (sh ? wt[3] : wt[1]) * sc;
#pragma unroll
    for (int ks = 0; ks < 2; ++ks) {
      v4u ob;
#pragma unroll
      for (int u = 0; u < 4; ++u) ob[u] = cvtpk(fmaf(w0, u2f(og[ks][0][u].x), w1 * u2f(og[ks][1][u].x)), fmaf(w0, u2f(og[ks][0][u].z), w1 * u2f(og[ks][1][u].z)));
#pragma unroll
      for (int d = 0; d < 8; ++d) dacc[d] = mfma(wf[ks * 8 + d], ob, dacc[d]);
    }
#pragma unroll
    for (int d = 0; d < 8; ++d)
#pragma unroll
      for (int g = 0; g < 4; ++g) dacc[d][g] += ror8(dacc[d][g]);
#pragma unroll
    for (int g = 0; g < 4; ++g)
#pragma unroll
      for (int e = 0; e < 2; ++e) {
        const float v0 = sh ? dacc[4 + 2 * e][g] : dacc[2 * e][g], v1 = sh ? dacc[5 + 2 * e][g] : dacc[2 * e + 1][g];
        const int cp = cg * 64 + 4 * (4 * h + g) + 2 * sh + e;
        __builtin_amdgcn_raw_buffer_store_b128(v4u{f2u(v0), tag, f2u(v1), tag}, rw, WS_P3 + ((cp * 64 + unit) * 8 + bb) * 16, 0, AUX_SC1);
      }
    STAMP(7);
  } else if (w == 1) {
    const int po = WS_P3 + bid * 8192 + lane * 16;
    v4u pg[8];
    for (unsigned spins = 0; spins < SPIN_CAP; ++spins) {
      asm volatile("" ::: "memory");
      bool ok = true;
#pragma unroll
      for (int i = 0; i < 8; ++i) pg[i] = __builtin_amdgcn_raw_buffer_load_b128(rw, po + i * 1024, 0, AUX_SC1);
#pragma unroll
      for (int i = 0; i < 8; ++i) ok = ok && pg[i].y == tag && pg[i].w == tag;
      if (__all(ok)) break;
    }
    float s0 = 0.f, s1 = 0.f;
#pragma unroll
    for (int i = 0; i < 8; ++i) { s0 += u2f(pg[i].x); s1 += u2f(pg[i].z); }
    s0 += ror8(s0); s1 += ror8(s1);
    s0 = xrow_sum(s0); s1 = xrow_sum(s1);
    if (lane < 8) out[lane * (DM / 2) + bid] = cvtpk(s0, s1);
    if (bid == 0 && lane == 0) __builtin_amdgcn_raw_buffer_store_b32(tag, rw, WS_E, 0, AUX_SC1);
    STAMP(7);
  }
}

extern "C" void kernel_launch(void* const* d_in, const int* in_sizes, int n_in, void* d_out, int out_size, void* d_ws, size_t ws_size, hipStream_t stream) {
  k_fused<<<256, 512, 0, stream>>>((const u16*)d_in[0], (const u16*)d_in[1], (const u16*)d_in[2], (const u16*)d_in[3], (const u16*)d_in[4],
                                   (const u16*)d_in[6], (const u16*)d_in[7], (const u16*)d_in[8], (const u16*)d_in[9], (unsigned char*)d_ws, (unsigned*)d_out);
}
```

```cpp
#include <hip/hip_runtime.h>
#include <hip/hip_bf16.h>
#include <cstdint>

#define LAS __attribute__((address_space(3)))
typedef unsigned v4u __attribute__((ext_vector_type(4)));
typedef unsigned v2u __attribute__((ext_vector_type(2)));
typedef float f32x4 __attribute__((ext_vector_type(4)));
typedef float f32x2 __attribute__((ext_vector_type(2)));
typedef short bf16x8 __attribute__((ext_vector_type(8)));
typedef unsigned short u16;

#define PACE 5
#define LIM 0
#define NP3 2
#define LATEW 4
#define T2 3
#define T3 1
#define SL2 8
#define SL3 8
#define WARM 1
#define ROT 6
#define STR_(x) #x
#define STR(x) STR_(x)

constexpr int NB = 8, DM = 512, HQ = 32, HKV = 8, SK = 8192, DH = 128, FF = 1792;
constexpr int NSPLIT = 4, NSW = 4, TILES_WG = SK / NSPLIT / 32, TPW = TILES_WG / NSW, NTHR = 64 * (1 + NSW);
constexpr float C2 = 0.088388347648318440f * 1.4426950408889634f;
constexpr int AUX_NT = 2, AUX_SC1 = 16, RS3 = 0x00020000;
constexpr unsigned SPIN_CAP = 1u << 18, LSPIN_CAP = 1u << 22;

constexpr int WS_E = 0;
constexpr int WS_QG = 4096;
constexpr int WS_GP = WS_QG + 8 * 8 * 4 * 512 * 8;
constexpr int WS_PO = WS_GP + 64 * 2 * 4 * 8 * 28 * 8;
constexpr int WS_ML = WS_PO + 32 * 8 * 4 * 128 * 8;
constexpr int WS_P3 = WS_ML + 32 * 8 * 4 * 2 * 8;
constexpr int WS_END = WS_P3 + 256 * 64 * 16 * 8;
constexpr int WS_ST = WS_END;
#define STAMP(i) do { } while (0)
#define STAMPT(i, t) do { } while (0)
#define NOW() 0ull

constexpr int L_QT = 0;
constexpr int L_QS = L_QT + 4096;
constexpr int L_LO = L_QS + 1024;
constexpr int L_ML = L_LO + 4 * 2048;
constexpr int L_HS = L_ML + 128;
constexpr int L_MISC = L_HS + 512;
constexpr int L_END = L_MISC + 32;

__device__ __forceinline__ f32x4 mfma(v4u a, v4u b, f32x4 c) {
  return __builtin_amdgcn_mfma_f32_16x16x32_bf16(__builtin_bit_cast(bf16x8, a), __builtin_bit_cast(bf16x8, b), c, 0, 0, 0);
}
__device__ __forceinline__ unsigned cvtpk(float lo, float hi) {
  typedef __bf16 bf16x2_ __attribute__((ext_vector_type(2)));
  return __builtin_bit_cast(unsigned, __builtin_convertvector((f32x2{lo, hi}), bf16x2_));
}
__device__ __forceinline__ float u2f(unsigned u) { return __builtin_bit_cast(float, u); }
__device__ __forceinline__ unsigned f2u(float f) { return __builtin_bit_cast(unsigned, f); }
__device__ __forceinline__ float rbf(float x) { return u2f(cvtpk(x, 0.f) << 16); }
__device__ __forceinline__ float xrow_max(float x) {
  auto s = __builtin_amdgcn_permlane16_swap(f2u(x), f2u(x), false, false);
  x = fmaxf(u2f(s[0]), u2f(s[1]));
  auto t = __builtin_amdgcn_permlane32_swap(f2u(x), f2u(x), false, false);
  return fmaxf(u2f(t[0]), u2f(t[1]));
}
__device__ __forceinline__ float xrow_sum(float x) {
  auto s = __builtin_amdgcn_permlane16_swap(f2u(x), f2u(x), false, false);
  x = u2f(s[0]) + u2f(s[1]);
  auto t = __builtin_amdgcn_permlane32_swap(f2u(x), f2u(x), false, false);
  return u2f(t[0]) + u2f(t[1]);
}
__device__ __forceinline__ float ror8(float x) { return u2f((unsigned)__builtin_amdgcn_mov_dpp((int)f2u(x), 0x128, 0xf, 0xf, true)); }
__device__ __forceinline__ float sigmoidf(float x) { return 1.f / (1.f + __expf(-x)); }
__device__ __forceinline__ v4u tr_frag(const v4u (&v)[8], int d) {
  v4u a;
#pragma unroll
  for (int u = 0; u < 4; ++u) a[u] = __builtin_amdgcn_perm(v[2 * u + 1][d >> 1], v[2 * u][d >> 1], (d & 1) ? 0x07060302u : 0x05040100u);
  return a;
}
#define SB() do { asm volatile("" ::: "memory"); __builtin_amdgcn_sched_barrier(0); } while (0)
#define TAG_DIFF(g) (((g).y ^ tag) | ((g).w ^ tag))
#define LDS_WAIT_GE(idx, val, slp) do { _Pragma("nounroll") for (unsigned sp_ = 0; sp_ < LSPIN_CAP; ++sp_) { if (__builtin_amdgcn_readfirstlane(misc[idx]) >= (unsigned)(val)) break; if (slp) __builtin_amdgcn_s_sleep(slp); } asm volatile("" ::: "memory"); } while (0)
#define LDS_SIGNAL(idx) do { asm volatile("s_waitcnt lgkmcnt(0)" ::: "memory"); if (lane == 0) __hip_atomic_fetch_add((LAS unsigned*)(L + L_MISC) + (idx), 1u, __ATOMIC_RELAXED, __HIP_MEMORY_SCOPE_WORKGROUP); } while (0)

__device__ __forceinline__ void proj_block(const v4u (&wr)[2][8], const v4u (&xb)[2], LAS unsigned char* L, int lane, f32x2 (&sums)[4]) {
  const int r = lane & 15, h = lane >> 4;
  f32x4 dq[8];
#pragma unroll
  for (int d = 0; d < 8; ++d) dq[d] = mfma(tr_frag(wr[1], d), xb[1], mfma(tr_frag(wr[0], d), xb[0], f32x4{0, 0, 0, 0}));
  if ((h >> 1) == (r >> 3)) {
#pragma unroll
    for (int g = 0; g < 4; ++g) {
      LAS float* dst = (LAS float*)(L + L_QT) + r * 64 + 32 * (h & 1) + 8 * g;
      *(LAS f32x4*)dst = f32x4{dq[0][g], dq[1][g], dq[2][g], dq[3][g]};
      *(LAS f32x4*)(dst + 4) = f32x4{dq[4][g], dq[5][g], dq[6][g], dq[7][g]};
    }
  }
  asm volatile("s_waitcnt lgkmcnt(0)" ::: "memory");
#pragma unroll
  for (int i = 0; i < 4; ++i) sums[i] = *(const LAS f32x2*)(L + L_QT + i * 512 + lane * 8) + *(const LAS f32x2*)(L + L_QT + 2048 + i * 512 + lane * 8);
  asm volatile("s_waitcnt lgkmcnt(0)" : "+v"(sums[0]), "+v"(sums[1]), "+v"(sums[2]), "+v"(sums[3]) :: "memory");
}

__global__ void __launch_bounds__(NTHR, 2) k_fused(const u16* __restrict__ x, const u16* Kc, const u16* Vc, const u16* wq,
                                                   const u16* wo, const u16* wg, const u16* wu, const u16* wd,
                                                   const u16* __restrict__ gw, unsigned char* ws, unsigned* out) {
  __shared__ __attribute__((aligned(16))) unsigned char smem[L_END];
  LAS unsigned char* L = (LAS unsigned char*)smem;
  volatile LAS unsigned* misc = (volatile LAS unsigned*)(L + L_MISC);
  const int tid = threadIdx.x, lane = tid & 63, w = __builtin_amdgcn_readfirstlane(tid >> 6);
  const int bid = blockIdx.x, kvh = bid & 7, j = bid >> 3, b = j >> 2, sp = j & 3;
  const int r = lane & 15, h = lane >> 4;
  STAMP(0);
  const __amdgpu_buffer_rsrc_t rw = __builtin_amdgcn_make_buffer_rsrc((void*)ws, 0, WS_END, RS3);
  if (w == 0) {
    __builtin_amdgcn_s_setprio(3);
    const int unit = kvh * 8 + b, bb = r & 7, sh = r >> 3;
    unsigned tag;
    {
      const __amdgpu_buffer_rsrc_t rq = __builtin_amdgcn_make_buffer_rsrc((void*)wq, 0, DM * 4096 * 2, RS3);
      const int row = 128 * sp + 8 * h + 32 * sh;
      const int qoff = row * 8192 + (64 * unit + 8 * bb) * 2;
      v4u wr[2][8], gr[2][8], xb[2];
#pragma unroll
      for (int s = 0; s < 2; ++s)
#pragma unroll
        for (int i = 0; i < 8; ++i) wr[s][i] = __builtin_amdgcn_raw_buffer_load_b128(rq, qoff, (64 * s + i) * 8192, AUX_NT);
#pragma unroll
      for (int s = 0; s < 2; ++s) xb[s] = *(const v4u*)(x + bb * DM + row + 64 * s);
      const unsigned epoch = __builtin_amdgcn_raw_buffer_load_b32(rw, WS_E, 0, AUX_SC1);
      SB();
      const int mat = unit >= 28 ? 1 : 0, cb = unit - 28 * mat;
      const __amdgpu_buffer_rsrc_t rg = __builtin_amdgcn_make_buffer_rsrc((void*)(mat ? wu : wg), 0, unit < 56 ? DM * FF * 2 : 0, RS3);
      const int goff = row * (FF * 2) + (64 * cb + 8 * bb) * 2;
#pragma unroll
      for (int s = 0; s < 2; ++s)
#pragma unroll
        for (int i = 0; i < 8; ++i) gr[s][i] = __builtin_amdgcn_raw_buffer_load_b128(rg, goff, (64 * s + i) * (FF * 2), AUX_NT);
      if (lane < 8) misc[lane] = 0u;
      SB();
      __syncthreads();
      SB();
      f32x2 sums[4];
      proj_block(wr, xb, L, lane, sums);
      STAMP(1);
      tag = __builtin_amdgcn_readfirstlane(epoch) + 1u;
#pragma unroll
      for (int i = 0; i < 4; ++i)
        __builtin_amdgcn_raw_buffer_store_b128(v4u{f2u(sums[i].x), tag, f2u(sums[i].y), tag}, rw, WS_QG + (((kvh * 8 + 2 * i + (lane >> 5)) * 4 + sp) * 512 + 64 * b + 2 * (lane & 31)) * 8, 0, AUX_SC1);
      SB();
      proj_block(gr, xb, L, lane, sums);
      if (unit < 56) {
        const int cp = 32 * cb + (lane & 31), cu = cp / 14, pos = cp - 14 * cu;
#pragma unroll
        for (int i = 0; i < 4; ++i)
          __builtin_amdgcn_raw_buffer_store_b128(v4u{f2u(sums[i].x), tag, f2u(sums[i].y), tag}, rw, WS_GP + ((((cu * 2 + mat) * 4 + sp) * 8 + 2 * i + (lane >> 5)) * 14 + pos) * 16, 0, AUX_SC1);
      }
      SB();
      STAMP(2);
    }
    {
      const int qo = WS_QG + unit * 16384 + lane * 16;
      v4u qg[16];
      unsigned done = 0;
#pragma nounroll
      for (unsigned spins = 0; spins < SPIN_CAP; ++spins) {
        asm volatile("" ::: "memory");
#pragma unroll
        for (int i = 0; i < 4; ++i) if (!((done >> i) & 1u)) {
#pragma unroll
          for (int u = 0; u < 4; ++u) qg[4 * i + u] = __builtin_amdgcn_raw_buffer_load_b128(rw, qo + (4 * i + u) * 1024, 0, AUX_SC1);
        }
#pragma unroll
        for (int i = 0; i < 4; ++i) if (__all((TAG_DIFF(qg[4 * i]) | TAG_DIFF(qg[4 * i + 1]) | TAG_DIFF(qg[4 * i + 2]) | TAG_DIFF(qg[4 * i + 3])) == 0u)) done |= 1u << i;
        if (done == 15u) break;
      }
#pragma unroll
      for (int i = 0; i < 4; ++i)
        *(LAS unsigned*)(L + L_QS + i * 256 + lane * 4) = cvtpk((u2f(qg[i].x) + u2f(qg[4 + i].x)) + (u2f(qg[8 + i].x) + u2f(qg[12 + i].x)), (u2f(qg[i].z) + u2f(qg[4 + i].z)) + (u2f(qg[8 + i].z) + u2f(qg[12 + i].z)));
      misc[1] = tag;
      asm volatile("s_waitcnt lgkmcnt(0)" ::: "memory");
      misc[0] = 1u;
      STAMP(3);
    }
    float gmix; v4u hb;
    {
      f32x4 ga = {0, 0, 0, 0};
      v4u xf[8], gf[8];
#pragma unroll
      for (int t = 0; t < 8; ++t) { xf[t] = *(const v4u*)(x + bb * DM + 64 * t + 32 * sh + 8 * h); gf[t] = *(const v4u*)(gw + 64 * t + 32 * sh + 8 * h); }
      const int go = WS_GP + unit * 14336 + min(lane, 55) * 16;
      v4u gg[16];
#pragma nounroll
      for (unsigned spins = 0; spins < SPIN_CAP; ++spins) {
        asm volatile("" ::: "memory");
        unsigned df = 0;
#pragma unroll
        for (int i = 0; i < 16; ++i) gg[i] = __builtin_amdgcn_raw_buffer_load_b128(rw, go + i * 896, 0, AUX_SC1);
#pragma unroll
        for (int i = 0; i < 16; ++i) df |= TAG_DIFF(gg[i]);
        if (__all(df == 0u)) break;
        __builtin_amdgcn_s_sleep(16);
      }
#pragma unroll
      for (int t = 0; t < 8; ++t) ga = mfma(gf[t], xf[t], ga);
      float gs = xrow_sum(h == 2 * sh ? ga[0] : 0.f);
      gs += ror8(gs);
      gmix = rbf(sigmoidf(rbf(gs)));
      *(LAS v2u*)(L + L_HS + lane * 8) = v2u{0u, 0u};
#pragma unroll
      for (int bh = 0; bh < 2; ++bh) {
        const float g0 = (u2f(gg[bh].x) + u2f(gg[2 + bh].x)) + (u2f(gg[4 + bh].x) + u2f(gg[6 + bh].x)), g1 = (u2f(gg[bh].z) + u2f(gg[2 + bh].z)) + (u2f(gg[4 + bh].z) + u2f(gg[6 + bh].z));
        const float u0 = (u2f(gg[8 + bh].x) + u2f(gg[10 + bh].x)) + (u2f(gg[12 + bh].x) + u2f(gg[14 + bh].x)), u1 = (u2f(gg[8 + bh].z) + u2f(gg[10 + bh].z)) + (u2f(gg[12 + bh].z) + u2f(gg[14 + bh].z));
        const int l14 = lane / 14;
        if (lane < 56) *(LAS unsigned*)(L + L_HS + (4 * bh + l14) * 64 + (lane - 14 * l14) * 4) = cvtpk(rbf(sigmoidf(rbf(g0))) * rbf(u0), rbf(sigmoidf(rbf(g1))) * rbf(u1));
      }
      SB();
      hb = *(const LAS v4u*)(L + L_HS + bb * 64 + h * 16);
      asm volatile("" : "+v"(hb), "+v"(gmix));
    }
#define WAUX_ AUX_SC1
#define WARM_TOUCH() do { \
      const int so_ = WS_P3 + (((sp * 64 + 16 * h + 2 * sh) * 64 + unit) * 8 + bb) * 16; \
      unsigned acc_ = 0u; \
      _Pragma("unroll") for (int i = 0; i < 8; ++i) acc_ |= __builtin_amdgcn_raw_buffer_load_b32(rw, so_, (4 * (i >> 1) + (i & 1)) * 8192, WAUX_); \
      _Pragma("unroll") for (int i = 0; i < 4; ++i) acc_ |= __builtin_amdgcn_raw_buffer_load_b32(rw, WS_PO + ((kvh * 4 + i) * 2 + (lane >> 5)) * 16384 + ((sp & 1) * 8 + ((lane >> 2) & 7)) * 1024 + b * 128 + (sp >> 1) * 64 + (lane & 3) * 16, 0, WAUX_); \
      acc_ |= __builtin_amdgcn_raw_buffer_load_b32(rw, WS_ML + (((kvh * 4 + (lane & 3)) * 8 + b) * NSPLIT + sp) * 16, 0, WAUX_); \
      acc_ |= __builtin_amdgcn_raw_buffer_load_b32(rw, WS_P3 + bid * 8192 + lane * 128, 0, WAUX_); \
      acc_ |= __builtin_amdgcn_raw_buffer_load_b32(rw, WS_PO + unit * 16384 + lane * 256, 0, WAUX_); \
      acc_ |= __builtin_nontemporal_load(out + (lane & 7) * (DM / 2) + bid); \
      asm volatile("" :: "v"(acc_)); } while (0)
    STAMP(4);
    if (WARM == 1) WARM_TOUCH();
    if (WARM == 2) { LDS_WAIT_GE(2, 1, 2); WARM_TOUCH(); }
    LDS_WAIT_GE(2, LATEW, 2);
    v4u wf[16]; f32x4 dacc[8];
    {
      v4u wr[2][8], dr[8];
      const u16* pd = wd + (size_t)(unit * 28 + 8 * h) * DM + sp * 128 + 8 * r;
#pragma unroll
      for (int i = 0; i < 8; ++i) { const bool ok = i < 4 || h < 3; dr[i] = __builtin_nontemporal_load((const v4u*)(pd + (ok ? i : 0) * DM)); }
      const u16* po = wo + (size_t)(unit * 64 + 8 * h) * DM + sp * 128 + 8 * r;
#pragma unroll
      for (int s = 0; s < 2; ++s)
#pragma unroll
        for (int i = 0; i < 8; ++i) wr[s][i] = __builtin_nontemporal_load((const v4u*)(po + (32 * s + i) * DM));
      SB();
#pragma unroll
      for (int i = 4; i < 8; ++i) dr[i] = h < 3 ? dr[i] : v4u{0u, 0u, 0u, 0u};
      const float omg = rbf(1.f - gmix);
#pragma unroll
      for (int d = 0; d < 8; ++d) dacc[d] = mfma(tr_frag(dr, d), hb, f32x4{0, 0, 0, 0}) * omg;
#pragma unroll
      for (int i = 0; i < 16; ++i) wf[i] = tr_frag(wr[i >> 3], i & 7);
#pragma unroll
      for (int i = 0; i < 16; ++i) asm volatile("" : "+v"(wf[i]));
#pragma unroll
      for (int d = 0; d < 8; ++d) asm volatile("" : "+v"(dacc[d]));
    }
    STAMP(5);
    {
      const int mo = WS_ML + (((unit >> 1) * 8 + bb) * NSPLIT + 2 * sh) * 16;
      const int oo = WS_PO + unit * 16384 + bb * 128 + sh * 64 + h * 16;
      int so = WS_P3 + (((sp * 64 + 16 * h + 2 * sh) * 64 + unit) * 8 + bb) * 16;
      asm volatile("" : "+v"(so));
      v4u mlg[2], og[2][2][4];
      unsigned dm[2] = {1u, 1u}, dd[2] = {1u, 1u};
      bool eager = false;
#pragma nounroll
      for (unsigned spins = 0; spins < SPIN_CAP; ++spins) {
        asm volatile("" ::: "memory");
        const bool w0_ = dd[0] != 0u && (dm[0] == 0u || eager), w1_ = dd[1] != 0u && (dm[1] == 0u || eager);
        const bool wm_ = (dm[0] | dm[1]) != 0u;
        if (wm_) { mlg[0] = __builtin_amdgcn_raw_buffer_load_b128(rw, mo, 0, AUX_SC1); mlg[1] = __builtin_amdgcn_raw_buffer_load_b128(rw, mo + 16, 0, AUX_SC1); }
        if (w0_) {
#pragma unroll
          for (int i = 0; i < 8; ++i) og[i >> 2][0][i & 3] = __builtin_amdgcn_raw_buffer_load_b128(rw, oo + (i & 3) * 1024, (i >> 2) * 4096, AUX_SC1);
        }
        if (w1_) {
#pragma unroll
          for (int i = 0; i < 8; ++i) og[i >> 2][1][i & 3] = __builtin_amdgcn_raw_buffer_load_b128(rw, oo + (i & 3) * 1024, 8192 + (i >> 2) * 4096, AUX_SC1);
        }
        SB();
        dm[0] = TAG_DIFF(mlg[0]); dm[1] = TAG_DIFF(mlg[1]);
        if (w0_) { unsigned t = 0u;
#pragma unroll
          for (int i = 0; i < 8; ++i) t |= TAG_DIFF(og[i >> 2][0][i & 3]);
          dd[0] = t; }
        if (w1_) { unsigned t = 0u;
#pragma unroll
          for (int i = 0; i < 8; ++i) t |= TAG_DIFF(og[i >> 2][1][i & 3]);
          dd[1] = t; }
        if (__ballot((dm[0] | dm[1] | dd[0] | dd[1]) != 0u) == 0ull) break;
        eager = __builtin_popcountll(__ballot(dm[0] != 0u)) + __builtin_popcountll(__ballot(dm[1] != 0u)) <= 4 * T2;
        if (!eager) __builtin_amdgcn_s_sleep(SL2);
      }
      const unsigned long long tg = NOW();
      float M = fmaxf(u2f(mlg[0].x), u2f(mlg[1].x));
      M = fmaxf(M, ror8(M));
      const float e0 = __builtin_amdgcn_exp2f(u2f(mlg[0].x) - M), e1 = __builtin_amdgcn_exp2f(u2f(mlg[1].x) - M);
      float Ls = fmaf(e0, u2f(mlg[0].z), e1 * u2f(mlg[1].z));
      Ls += ror8(Ls);
      const float sc = gmix * __builtin_amdgcn_rcpf(Ls);
      const float w0 = e0 * sc, w1 = e1 * sc;
      v4u ob[2];
#pragma unroll
      for (int ks = 0; ks < 2; ++ks)
#pragma unroll
        for (int u = 0; u < 4; ++u) {
          const float a0 = fmaf(w0, u2f(og[ks][0][u].x), w1 * u2f(og[ks][1][u].x)), a1 = fmaf(w0, u2f(og[ks][0][u].z), w1 * u2f(og[ks][1][u].z));
          ob[ks][u] = cvtpk(a0 + ror8(a0), a1 + ror8(a1));
        }
#pragma unroll
      for (int e = 0; e < 2; ++e) {
#pragma unroll
        for (int ks = 0; ks < 2; ++ks)
#pragma unroll
          for (int q = 0; q < 4; ++q) { const int d = 2 * e + (q & 1) + 4 * (q >> 1); dacc[d] = mfma(wf[ks * 8 + d], ob[ks], dacc[d]); }
      }
#pragma unroll
      for (int e = 0; e < 2; ++e)
#pragma unroll
        for (int g = 0; g < 4; ++g) {
          const float v0 = sh ? dacc[4 + 2 * e][g] : dacc[2 * e][g], v1 = sh ? dacc[5 + 2 * e][g] : dacc[2 * e + 1][g];
          __builtin_amdgcn_raw_buffer_store_b128(v4u{f2u(v0), tag, f2u(v1), tag}, rw, so, (4 * g + e) * 8192, AUX_SC1);
        }
      misc[4] = 1u;
      STAMP(6); STAMPT(7, tg);
    }
    return;
  }
  const int sw = w - 1;
  constexpr int t0 = 0, t1 = TPW;
  const size_t base = ((size_t)(b * HKV + kvh) * SK + (size_t)sp * (SK / NSPLIT)) * DH;
  constexpr int US = ROT == 5 ? 4096 : 1024, WS_ = ROT == 5 ? 1024 : 8192;
  const int kl = sw * WS_ + (r & 3) * US + (r >> 2) * 256 + h * 16, vl = sw * WS_ + lane * 16;
  v4u kb[2][8], vb[2][8];
#define PACED(rdy) asm volatile("s_cmp_lg_u32 %0, 0\n\ts_cbranch_scc1 1f\n\ts_waitcnt vmcnt(" STR(PACE) ")\n1:" :: "s"(rdy) : "scc", "memory")
#define ISSUE_K(d, t_, PC) do { const __amdgpu_buffer_rsrc_t rk_ = __builtin_amdgcn_make_buffer_rsrc((void*)(Kc + base), 0, (t_) < t1 ? SK * DH * 2 / NSPLIT : 0, RS3); const int so_ = ((t_) & 15) * 32768; \
    unsigned rdy_ = 1u; if (PC) rdy_ = __builtin_amdgcn_readfirstlane(misc[0]); \
    _Pragma("unroll") for (int i = 0; i < 8; ++i) { if (PC) { PACED(rdy_); SB(); } kb[d][i] = __builtin_amdgcn_raw_buffer_load_b128(rk_, kl + (i & 3) * 64, so_ + (i >> 2) * 4 * US, AUX_NT); if (PC) SB(); } SB(); } while (0)
#define ISSUE_V(d, t_, PC) do { const __amdgpu_buffer_rsrc_t rv_ = __builtin_amdgcn_make_buffer_rsrc((void*)(Vc + base), 0, (t_) < t1 ? SK * DH * 2 / NSPLIT : 0, RS3); const int so_ = ((t_) & 15) * 32768; \
    unsigned rdy_ = 1u; if (PC) rdy_ = __builtin_amdgcn_readfirstlane(misc[0]); \
    _Pragma("unroll") for (int i = 0; i < 8; ++i) { if (PC) { PACED(rdy_); SB(); } vb[d][i] = __builtin_amdgcn_raw_buffer_load_b128(rv_, vl, so_ + i * US, AUX_NT); if (PC) SB(); } SB(); } while (0)
  __syncthreads();
  ISSUE_K(0, t0, true); ISSUE_V(0, t0, true); ISSUE_K(1, t0 + 1, true); ISSUE_V(1, t0 + 1, true);
  STAMP(1);
  LDS_WAIT_GE(0, 1, 1);
  v4u qf[4];
#pragma unroll
  for (int i = 0; i < 4; ++i) qf[i] = *(const LAS v4u*)(L + L_QS + (r & 3) * 256 + i * 64 + h * 16);
  const unsigned tag = __builtin_amdgcn_readfirstlane(misc[1]);
  STAMP(2);
  f32x4 acc[8];
#pragma unroll
  for (int d = 0; d < 8; ++d) acc[d] = f32x4{0, 0, 0, 0};
  float m = -INFINITY, l = 0.f;
#define LIMIT() do { if (LIM == 1) asm volatile("s_waitcnt vmcnt(4)" ::: "memory"); else if (LIM == 2) asm volatile("s_waitcnt vmcnt(2)" ::: "memory"); else if (LIM == 3) asm volatile("s_waitcnt vmcnt(0)" ::: "memory"); } while (0)
#define STEP(d, tn, REFILL, LM) do { \
    f32x4 s0_ = {0, 0, 0, 0}, s1_ = {0, 0, 0, 0}; \
    _Pragma("unroll") for (int i = 0; i < 4; ++i) { s0_ = mfma(kb[d][i], qf[i], s0_); s1_ = mfma(kb[d][4 + i], qf[i], s1_); } \
    asm volatile("" : "+v"(s0_), "+v"(s1_)); \
    SB(); \
    if (REFILL && !(LM && LIM)) ISSUE_K(d, tn, false); \
    float tm = fmaxf(fmaxf(fmaxf(s0_[0], s0_[1]), fmaxf(s0_[2], s0_[3])), fmaxf(fmaxf(s1_[0], s1_[1]), fmaxf(s1_[2], s1_[3]))); \
    tm = xrow_max(tm) * C2; \
    const float mn = fmaxf(m, tm), alpha = __builtin_amdgcn_exp2f(m - mn); m = mn; \
    float p[8]; \
    _Pragma("unroll") for (int i = 0; i < 4; ++i) { p[i] = __builtin_amdgcn_exp2f(fmaf(s0_[i], C2, -mn)); p[4 + i] = __builtin_amdgcn_exp2f(fmaf(s1_[i], C2, -mn)); } \
    l = fmaf(l, alpha, ((p[0] + p[1]) + (p[2] + p[3])) + ((p[4] + p[5]) + (p[6] + p[7]))); \
    v4u pf = {cvtpk(p[0], p[1]), cvtpk(p[2], p[3]), cvtpk(p[4], p[5]), cvtpk(p[6], p[7])}; \
    _Pragma("unroll") for (int dd = 0; dd < 8; ++dd) acc[dd] *= alpha; \
    if (REFILL && LM && LIM) { asm volatile("" : "+v"(pf), "+v"(acc[0]), "+v"(acc[1]), "+v"(acc[2]), "+v"(acc[3]), "+v"(acc[4]), "+v"(acc[5]), "+v"(acc[6]), "+v"(acc[7])); SB(); LIMIT(); ISSUE_K(d, tn, false); } \
    _Pragma("unroll") for (int dd = 0; dd < 8; ++dd) acc[dd] = mfma(tr_frag(vb[d], dd), pf, acc[dd]); \
    asm volatile("" : "+v"(acc[0]), "+v"(acc[1]), "+v"(acc[2]), "+v"(acc[3]), "+v"(acc[4]), "+v"(acc[5]), "+v"(acc[6]), "+v"(acc[7])); \
    SB(); \
    if (REFILL) { if (LM) LIMIT(); ISSUE_V(d, tn, false); } \
  } while (0)
  STEP(0, t0 + 2, true, false); STEP(1, 0, false, false);
#pragma unroll 1
  for (int t = t0 + 3; t <= t1; ++t) { if (t == t1) LDS_SIGNAL(2); STEP(0, t, true, true); }
  STAMP(3);
  l = xrow_sum(l);
  if (r < 4) {
#pragma unroll
    for (int g = 0; g < 4; ++g) {
      LAS float* dst = (LAS float*)(L + L_LO) + (sw * 4 + r) * DH + 8 * (4 * h + g);
      *(LAS f32x4*)dst = f32x4{acc[0][g], acc[1][g], acc[2][g], acc[3][g]};
      *(LAS f32x4*)(dst + 4) = f32x4{acc[4][g], acc[5][g], acc[6][g], acc[7][g]};
    }
    if (h == 0) *(LAS f32x2*)(L + L_ML + (sw * 4 + r) * 8) = f32x2{m, l};
  }
  LDS_SIGNAL(3);
  {
    const int dm = (lane & 48) * 2 + (lane & 3) * 8 + ((lane >> 2) & 3) * 2;
    float M, Ls; f32x2 o;
#pragma nounroll
    for (unsigned spins = 0; spins < LSPIN_CAP; ++spins) {
      const unsigned arrived = misc[3];
      asm volatile("" ::: "memory");
      f32x2 ml[NSW];
      M = -INFINITY;
#pragma unroll
      for (int ww = 0; ww < NSW; ++ww) { ml[ww] = *(const LAS f32x2*)(L + L_ML + (ww * 4 + sw) * 8); M = fmaxf(M, ml[ww].x); }
      Ls = 0.f; o = f32x2{0.f, 0.f};
#pragma unroll
      for (int ww = 0; ww < NSW; ++ww) { const float wt = __builtin_amdgcn_exp2f(ml[ww].x - M); Ls = fmaf(wt, ml[ww].y, Ls); o += wt * *(const LAS f32x2*)(L + L_LO + ((ww * 4 + sw) * DH + dm) * 4); }
      asm volatile("" : "+v"(o), "+v"(M), "+v"(Ls) :: "memory");
      if (__builtin_amdgcn_readfirstlane(arrived) >= (unsigned)NSW) break;
    }
    const int head = kvh * 4 + sw;
    __builtin_amdgcn_raw_buffer_store_b128(v4u{f2u(o.x), tag, f2u(o.y), tag}, rw,
        WS_PO + (head * 2 + (lane >> 5)) * 16384 + ((sp & 1) * 8 + ((lane >> 2) & 7)) * 1024 + b * 128 + (sp >> 1) * 64 + (lane & 3) * 16, 0, AUX_SC1);
    if (lane == 0) __builtin_amdgcn_raw_buffer_store_b128(v4u{f2u(M), tag, f2u(Ls), tag}, rw, WS_ML + ((head * 8 + b) * NSPLIT + sp) * 16, 0, AUX_SC1);
    STAMP(6);
  }
  if (sw >= NP3) return;
  {
    LDS_WAIT_GE(4, 1, 1);
    if (sw) __builtin_amdgcn_s_sleep(NP3 == 2 ? 10 : 5);
    if (sw > 1) __builtin_amdgcn_s_sleep(5);
    if (sw > 2) __builtin_amdgcn_s_sleep(5);
    const int po = WS_P3 + bid * 8192 + lane * 16;
    v4u pg[8];
    unsigned df[8];
#pragma unroll
    for (int i = 0; i < 8; ++i) df[i] = 1u;
    unsigned open = 1u << kvh;
    const int pro = WS_P3 + bid * 8192 + (lane >> 3) * 1024 + b * 128 + (lane & 7) * 16;
#pragma nounroll
    for (unsigned spins = 0; spins < SPIN_CAP; ++spins) {
      asm volatile("" ::: "memory");
      v4u pr = {0u, tag, 0u, tag};
      if (!((open >> (lane >> 3)) & 1u)) pr = __builtin_amdgcn_raw_buffer_load_b128(rw, pro, 0, AUX_SC1);
#pragma unroll
      for (int i = 0; i < 8; ++i) if (((open >> i) & 1u) && df[i] != 0u) pg[i] = __builtin_amdgcn_raw_buffer_load_b128(rw, po + i * 1024, 0, AUX_SC1);
      SB();
      unsigned any = 0u;
#pragma unroll
      for (int i = 0; i < 8; ++i) { if ((open >> i) & 1u) df[i] = TAG_DIFF(pg[i]); any |= df[i]; }
      if (__ballot(any != 0u) == 0ull) break;
      if (NP3 > 1 && __builtin_amdgcn_readfirstlane(misc[5]) != 0u) { STAMP(7); return; }
      const unsigned long long hit = __ballot(TAG_DIFF(pr) == 0u);
      unsigned op = 0u;
#pragma unroll
      for (int i = 0; i < 8; ++i) op |= ((hit >> (8 * i)) & 1ull) ? 1u << i : 0u;
      open = __builtin_popcount(op) >= 8 - T3 ? 255u : op;
      if (open != 255u) __builtin_amdgcn_s_sleep(SL3);
    }
    float s0 = 0.f, s1 = 0.f;
#pragma unroll
    for (int i = 0; i < 8; ++i) { s0 += u2f(pg[i].x); s1 += u2f(pg[i].z); }
    s0 += ror8(s0); s1 += ror8(s1);
    s0 = xrow_sum(s0); s1 = xrow_sum(s1);
    if (lane < 8) out[lane * (DM / 2) + bid] = cvtpk(s0, s1);
    misc[5] = 1u;
    if (bid == 0 && lane == 0) __builtin_amdgcn_raw_buffer_store_b32(tag, rw, WS_E, 0, AUX_SC1);
    STAMP(7);
  }
}

extern "C" void kernel_launch(void* const* d_in, const int* in_sizes, int n_in, void* d_out, int out_size, void* d_ws, size_t ws_size, hipStream_t stream) {
  k_fused<<<256, NTHR, 0, stream>>>((const u16*)d_in[0], (const u16*)d_in[1], (const u16*)d_in[2], (const u16*)d_in[3], (const u16*)d_in[4],
                                    (const u16*)d_in[6], (const u16*)d_in[7], (const u16*)d_in[8], (const u16*)d_in[9], (unsigned char*)d_ws, (unsigned*)d_out);
}
```

```cpp
#include <hip/hip_runtime.h>
#include <hip/hip_bf16.h>
#include <cstdint>

#define LAS __attribute__((address_space(3)))
typedef unsigned v4u __attribute__((ext_vector_type(4)));
typedef unsigned v2u __attribute__((ext_vector_type(2)));
typedef float f32x4 __attribute__((ext_vector_type(4)));
typedef float f32x2 __attribute__((ext_vector_type(2)));
typedef short bf16x8 __attribute__((ext_vector_type(8)));
typedef unsigned short u16;

constexpr int DELTA = 428, N1 = 32, DELTA1 = 470, TS0 = 108;
constexpr int JIT = 27;
constexpr int NPRE = 4;
constexpr int WOP = 24;
constexpr int S2T = 44;
constexpr int S3D = 8;
constexpr int CWARM = 18;

constexpr int NB = 8, DM = 512, HKV = 8, SK = 8192, DH = 128, FF = 1792;
constexpr int NSW = 4, NTHR = 64 * (1 + NSW), RING = 2;
constexpr float C2 = 0.088388347648318440f * 1.4426950408889634f;
constexpr int AUX_NT = 2, AUX_SC1 = 16, RS3 = 0x00020000, OOB = 0x7ff00000;
constexpr unsigned SPIN_CAP = 1u << 18, LSPIN_CAP = 1u << 24;

constexpr int WS_E = 0;
constexpr int WS_QG = 4096;
constexpr int WS_GP = WS_QG + 64 * 4 * 2048;
constexpr int WS_MX = WS_GP + 64 * 8 * 896;
constexpr int WS_PO = WS_MX + 256 * 128;
constexpr int WS_P3 = WS_PO + 64 * 32 * 256;
constexpr int WS_END = WS_P3 + 128 * 64 * 128;

constexpr int L_QT = 0;
constexpr int L_QS = L_QT + 4096;
constexpr int L_LO = L_QS + 1024;
constexpr int L_ML = L_LO + 6144;
constexpr int L_GT = L_ML + 128;
constexpr int L_HS = L_GT + 128;
constexpr int L_MISC = L_HS + 512;
constexpr int L_OB = L_MISC + 64;
constexpr int L_S = L_OB + 1024;
constexpr int L_END = L_S + 64 * 512;
static_assert(L_MISC % 16 == 0 && L_S % 16 == 0 && L_ML % 16 == 0, "LDS alignment");

__device__ __forceinline__ f32x4 mfma(v4u a, v4u b, f32x4 c) {
  return __builtin_amdgcn_mfma_f32_16x16x32_bf16(__builtin_bit_cast(bf16x8, a), __builtin_bit_cast(bf16x8, b), c, 0, 0, 0);
}
__device__ __forceinline__ unsigned cvtpk(float lo, float hi) {
  typedef __bf16 bf16x2_ __attribute__((ext_vector_type(2)));
  return __builtin_bit_cast(unsigned, __builtin_convertvector((f32x2{lo, hi}), bf16x2_));
}
__device__ __forceinline__ float u2f(unsigned u) { return __builtin_bit_cast(float, u); }
__device__ __forceinline__ unsigned f2u(float f) { return __builtin_bit_cast(unsigned, f); }
__device__ __forceinline__ float rbf(float x) { return u2f(cvtpk(x, 0.f) << 16); }
__device__ __forceinline__ float xrow_max(float x) {
  auto s = __builtin_amdgcn_permlane16_swap(f2u(x), f2u(x), false, false);
  x = fmaxf(u2f(s[0]), u2f(s[1]));
  auto t = __builtin_amdgcn_permlane32_swap(f2u(x), f2u(x), false, false);
  return fmaxf(u2f(t[0]), u2f(t[1]));
}
__device__ __forceinline__ float xrow_sum(float x) {
  auto s = __builtin_amdgcn_permlane16_swap(f2u(x), f2u(x), false, false);
  x = u2f(s[0]) + u2f(s[1]);
  auto t = __builtin_amdgcn_permlane32_swap(f2u(x), f2u(x), false, false);
  return u2f(t[0]) + u2f(t[1]);
}
__device__ __forceinline__ float ror8(float x) { return u2f((unsigned)__builtin_amdgcn_mov_dpp((int)f2u(x), 0x128, 0xf, 0xf, true)); }
__device__ __forceinline__ float ror4(float x) { return u2f((unsigned)__builtin_amdgcn_mov_dpp((int)f2u(x), 0x124, 0xf, 0xf, true)); }
__device__ __forceinline__ float sigmoidf(float x) { return 1.f / (1.f + __expf(-x)); }
__device__ __forceinline__ v4u tr_frag(const v4u (&v)[8], int d) {
  v4u a;
#pragma unroll
  for (int u = 0; u < 4; ++u) a[u] = __builtin_amdgcn_perm(v[2 * u + 1][d >> 1], v[2 * u][d >> 1], (d & 1) ? 0x07060302u : 0x05040100u);
  return a;
}
__device__ __forceinline__ v4u pack4(f32x4 v, unsigned tag) {
  return v4u{__builtin_amdgcn_perm(f2u(v[0]), f2u(v[1]), 0x07060503u), __builtin_amdgcn_perm(f2u(v[1]), tag, 0x06050100u),
             __builtin_amdgcn_perm(f2u(v[2]), f2u(v[3]), 0x07060503u), __builtin_amdgcn_perm(f2u(v[3]), tag, 0x06050100u)};
}
__device__ __forceinline__ f32x4 unpack4(v4u g) {
  return f32x4{u2f(g.x & 0xffffff00u), u2f(__builtin_amdgcn_perm(g.x, g.y, 0x0403020cu)), u2f(g.z & 0xffffff00u), u2f(__builtin_amdgcn_perm(g.z, g.w, 0x0403020cu))};
}
#define SB() do { asm volatile("" ::: "memory"); __builtin_amdgcn_sched_barrier(0); } while (0)
#define TAG_DIFF(g) (((g).y ^ tag) | ((g).w ^ tag))
#define LDS_WAIT_GE(idx, val, slp) do { _Pragma("nounroll") for (unsigned sp_ = 0; sp_ < LSPIN_CAP; ++sp_) { if (__builtin_amdgcn_readfirstlane(misc[idx]) >= (unsigned)(val)) break; if (slp) __builtin_amdgcn_s_sleep(slp); } asm volatile("" ::: "memory"); } while (0)
#define LDS_SIGNAL(idx) do { if (lane == 0) __hip_atomic_fetch_add((LAS unsigned*)(L + L_MISC) + (idx), 1u, __ATOMIC_RELAXED, __HIP_MEMORY_SCOPE_WORKGROUP); } while (0)
#define CLOCK_WAIT(t) do { _Pragma("nounroll") for (int sp_ = 0; sp_ < 8192; ++sp_) { if ((int)((unsigned)__builtin_amdgcn_s_memrealtime() - (t)) >= 0) break; __builtin_amdgcn_s_sleep(1); } } while (0)

__device__ __forceinline__ void proj_block(const v4u (&wr)[2][8], const v4u (&xb)[2], LAS unsigned char* L, int lane, f32x4 (&sums)[2]) {
  const int r = lane & 15, h = lane >> 4;
  f32x4 dq[8];
#pragma unroll
  for (int d = 0; d < 8; ++d) dq[d] = mfma(tr_frag(wr[1], d), xb[1], mfma(tr_frag(wr[0], d), xb[0], f32x4{0, 0, 0, 0}));
  if ((h >> 1) == (r >> 3)) {
#pragma unroll
    for (int g = 0; g < 4; ++g) {
      LAS float* dst = (LAS float*)(L + L_QT) + r * 64 + 32 * (h & 1) + 8 * g;
      *(LAS f32x4*)dst = f32x4{dq[0][g], dq[1][g], dq[2][g], dq[3][g]};
      *(LAS f32x4*)(dst + 4) = f32x4{dq[4][g], dq[5][g], dq[6][g], dq[7][g]};
    }
  }
  asm volatile("" ::: "memory");
#pragma unroll
  for (int i = 0; i < 2; ++i) sums[i] = *(const LAS f32x4*)(L + L_QT + i * 1024 + lane * 16) + *(const LAS f32x4*)(L + L_QT + 2048 + i * 1024 + lane * 16);
  asm volatile("" : "+v"(sums[0]), "+v"(sums[1]) :: "memory");
}

__global__ void __launch_bounds__(NTHR, 2) k_fused(const u16* __restrict__ x, const u16* Kc, const u16* Vc, const u16* wq,
                                                   const u16* wo, const u16* wg, const u16* wu, const u16* wd,
                                                   const u16* __restrict__ gw, unsigned char* ws, unsigned* out) {
  __shared__ __attribute__((aligned(16))) unsigned char smem[L_END];
  LAS unsigned char* L = (LAS unsigned char*)smem;
  volatile LAS unsigned* misc = (volatile LAS unsigned*)(L + L_MISC);
  const int tid = threadIdx.x, lane = tid & 63, w = __builtin_amdgcn_readfirstlane(tid >> 6);
  const int bid = blockIdx.x, kvh = bid & 7, j = bid >> 3, b = j >> 2, sp = j & 3;
  const int r = lane & 15, h = lane >> 4, bb = r & 7, sh = r >> 3, unit = kvh * 8 + b;
  unsigned long long pc0 = __builtin_amdgcn_s_getpc();
  unsigned tstart = (unsigned)__builtin_amdgcn_s_memrealtime();
  asm volatile("" : "+s"(pc0), "+s"(tstart));
  const __amdgpu_buffer_rsrc_t rw = __builtin_amdgcn_make_buffer_rsrc((void*)ws, 0, WS_END, RS3);
  if (__builtin_expect(w == 0, 1)) {
    unsigned tag;
    {
      const __amdgpu_buffer_rsrc_t rq = __builtin_amdgcn_make_buffer_rsrc((void*)wq, 0, DM * 4096 * 2, RS3);
      const int row = 128 * sp + 8 * h + 32 * sh;
      const int qoff = row * 8192 + (64 * unit + 8 * bb) * 2;
      v4u wr[2][8], gr[2][8], xb[2];
#pragma unroll
      for (int s = 0; s < 2; ++s)
#pragma unroll
        for (int i = 0; i < 8; ++i) wr[s][i] = __builtin_amdgcn_raw_buffer_load_b128(rq, qoff, (64 * s + i) * 8192, AUX_NT);
#pragma unroll
      for (int s = 0; s < 2; ++s) xb[s] = *(const v4u*)(x + bb * DM + row + 64 * s);
      const unsigned epoch = __builtin_amdgcn_raw_buffer_load_b32(rw, WS_E, 0, AUX_SC1);
      SB();
      const int mat = unit >= 28 ? 1 : 0, cb = unit - 28 * mat;
      const __amdgpu_buffer_rsrc_t rg = __builtin_amdgcn_make_buffer_rsrc((void*)(mat ? wu : wg), 0, unit < 56 ? DM * FF * 2 : 0, RS3);
      const int goff = row * (FF * 2) + (64 * cb + 8 * bb) * 2;
#pragma unroll
      for (int s = 0; s < 2; ++s)
#pragma unroll
        for (int i = 0; i < 8; ++i) gr[s][i] = __builtin_amdgcn_raw_buffer_load_b128(rg, goff, (64 * s + i) * (FF * 2), AUX_NT);
      if (lane < 16) misc[lane] = 0u;
      SB();
      __syncthreads();
      SB();
      f32x4 sums[2];
      proj_block(wr, xb, L, lane, sums);
      tag = __builtin_amdgcn_readfirstlane(epoch) * 1664525u + 1013904223u;
#pragma unroll
      for (int i = 0; i < 2; ++i) __builtin_amdgcn_raw_buffer_store_b128(pack4(sums[i], tag), rw, WS_QG + ((kvh * 8 + 4 * i + h) * 4 + sp) * 2048 + b * 256 + r * 16, 0, AUX_SC1);
      SB();
      proj_block(gr, xb, L, lane, sums);
      const int cq = 16 * cb + r, cu = cq / 7;
      const int gpo = unit < 56 ? WS_GP + ((cu * 2 + mat) * 4 + sp) * 896 + h * 112 + (cq - 7 * cu) * 16 : OOB;
#pragma unroll
      for (int i = 0; i < 2; ++i) __builtin_amdgcn_raw_buffer_store_b128(pack4(sums[i], tag), rw, gpo + i * 448, 0, AUX_SC1);
      SB();
    }
    {
      const int qo = WS_QG + unit * 8192 + lane * 16;
      v4u qg[8];
      unsigned done = 0;
#pragma nounroll
      for (unsigned spins = 0; spins < SPIN_CAP; ++spins) {
        asm volatile("" ::: "memory");
#pragma unroll
        for (int i = 0; i < 4; ++i) if (!((done >> i) & 1u)) {
#pragma unroll
          for (int u = 0; u < 2; ++u) qg[2 * i + u] = __builtin_amdgcn_raw_buffer_load_b128(rw, qo + u * 1024, i * 2048, AUX_SC1);
        }
#pragma unroll
        for (int i = 0; i < 4; ++i) if (__all(((TAG_DIFF(qg[2 * i]) | TAG_DIFF(qg[2 * i + 1])) & 0xffffu) == 0u)) done |= 1u << i;
        if (done == 15u) break;
      }
#pragma unroll
      for (int u = 0; u < 2; ++u) {
        const f32x4 q4 = (unpack4(qg[u]) + unpack4(qg[2 + u])) + (unpack4(qg[4 + u]) + unpack4(qg[6 + u]));
        *(LAS v2u*)(L + L_QS + u * 512 + lane * 8) = v2u{cvtpk(q4[0], q4[1]), cvtpk(q4[2], q4[3])};
      }
      misc[1] = tag;
      asm volatile("" ::: "memory");
      misc[0] = 1u;
    }
    v4u hb;
    {
      const int go = WS_GP + unit * 7168 + min(lane, 55) * 16;
      v4u gg[8];
#pragma nounroll
      for (unsigned spins = 0; spins < SPIN_CAP; ++spins) {
        asm volatile("" ::: "memory");
        unsigned df = 0;
#pragma unroll
        for (int i = 0; i < 8; ++i) gg[i] = __builtin_amdgcn_raw_buffer_load_b128(rw, go + (i & 3) * 896, (i >> 2) * 3584, AUX_SC1);
#pragma unroll
        for (int i = 0; i < 8; ++i) df |= TAG_DIFF(gg[i]);
        if (__all((df & 0xffffu) == 0u)) break;
        __builtin_amdgcn_s_sleep(16);
      }
      const f32x4 g4 = (unpack4(gg[0]) + unpack4(gg[1])) + (unpack4(gg[2]) + unpack4(gg[3])), u4 = (unpack4(gg[4]) + unpack4(gg[5])) + (unpack4(gg[6]) + unpack4(gg[7]));
      float hv[4];
#pragma unroll
      for (int i = 0; i < 4; ++i) hv[i] = rbf(sigmoidf(rbf(g4[i]))) * rbf(u4[i]);
      const int l7 = lane / 7;
      *(LAS v2u*)(L + L_HS + (lane < 56 ? l7 * 64 + (lane - 7 * l7) * 8 : (lane - 56) * 64 + 56)) = lane < 56 ? v2u{cvtpk(hv[0], hv[1]), cvtpk(hv[2], hv[3])} : v2u{0u, 0u};
      SB();
      hb = *(const LAS v4u*)(L + L_HS + bb * 64 + h * 16);
      asm volatile("" : "+v"(hb));
    }
    float gmix;
    {
      LDS_WAIT_GE(8, NSW, 4);
      const f32x4 gt = *(const LAS f32x4*)(L + L_GT + bb * 16);
      gmix = rbf(sigmoidf(rbf((gt[0] + gt[1]) + (gt[2] + gt[3]))));
      const f32x4 mw = *(const LAS f32x4*)(L + L_ML + (lane & 3) * 16);
      const float Mw = fmaxf(fmaxf(mw[0], mw[1]), fmaxf(mw[2], mw[3]));
      float ls = 0.f;
#pragma unroll 1
      for (int it = 0; it < 16; ++it) {
        const f32x4 s0 = *(const LAS f32x4*)(L + L_S + it * 2048 + lane * 32), s1 = *(const LAS f32x4*)(L + L_S + it * 2048 + lane * 32 + 16);
        float p[8];
#pragma unroll
        for (int i = 0; i < 4; ++i) { p[i] = __builtin_amdgcn_exp2f(fmaf(s0[i], C2, -Mw)); p[4 + i] = __builtin_amdgcn_exp2f(fmaf(s1[i], C2, -Mw)); }
        ls += ((p[0] + p[1]) + (p[2] + p[3])) + ((p[4] + p[5]) + (p[6] + p[7]));
      }
      ls += ror4(ls); ls += ror8(ls); ls = xrow_sum(ls);
      const int xo = WS_MX + (b * 8 + kvh) * 512;
      __builtin_amdgcn_raw_buffer_store_b128(v4u{f2u(Mw), tag, f2u(ls), tag}, rw, lane < 8 ? xo + sp * 128 + lane * 16 : OOB, 0, AUX_SC1);
      v4u xg;
#pragma nounroll
      for (unsigned spins = 0; spins < SPIN_CAP; ++spins) {
        __builtin_amdgcn_s_sleep(32);
        asm volatile("" ::: "memory");
        xg = __builtin_amdgcn_raw_buffer_load_b128(rw, xo + h * 128 + (lane & 3) * 16, 0, AUX_SC1);
        if (__all(TAG_DIFF(xg) == 0u)) break;
      }
      const float M = xrow_max(u2f(xg.x));
      const float Lt = xrow_sum(__builtin_amdgcn_exp2f(u2f(xg.x) - M) * u2f(xg.z));
      const float sc = u2f(__builtin_amdgcn_readlane(f2u(gmix), b)) / Lt;
      if (lane < 4) *(LAS f32x4*)(L + L_ML + 64 + lane * 16) = f32x4{__builtin_amdgcn_exp2f(mw[0] - M) * sc, __builtin_amdgcn_exp2f(mw[1] - M) * sc, __builtin_amdgcn_exp2f(mw[2] - M) * sc, __builtin_amdgcn_exp2f(mw[3] - M) * sc};
      asm volatile("" ::: "memory");
      misc[9] = 1u;
    }
    v4u wf[16]; f32x4 dacc[8];
    int so[4];
#pragma unroll
    for (int g = 0; g < 4; ++g) so[g] = WS_P3 + ((sp * 32 + 8 * h + 2 * g + sh) * 64 + unit) * 128 + bb * 16;
    {
      v4u wr[2][8], dr[8];
      const __amdgpu_buffer_rsrc_t rd = __builtin_amdgcn_make_buffer_rsrc((void*)wd, 0, FF * DM * 2, RS3);
      const __amdgpu_buffer_rsrc_t ro = __builtin_amdgcn_make_buffer_rsrc((void*)wo, 0, 4096 * DM * 2, RS3);
      const int od = (unit * 28 + 8 * h) * 1024 + sp * 256 + 16 * r, od2 = h < 3 ? od + 4096 : OOB;
      const int oo_ = (unit * 64 + 2 * h) * 1024 + sp * 256 + 16 * r;
      LDS_WAIT_GE(2, NSW, 1);
      const unsigned tw0 = (unsigned)__builtin_amdgcn_s_memrealtime() + WOP;
      CLOCK_WAIT(tw0);
      SB();
#pragma unroll
      for (int i = 0; i < 8; ++i) dr[i] = __builtin_amdgcn_raw_buffer_load_b128(rd, i < 4 ? od : od2, (i & 3) * 1024, AUX_NT);
      SB();
      CLOCK_WAIT(tw0 + 24);
      SB();
#pragma unroll
      for (int i = 0; i < 8; ++i) wr[0][i] = __builtin_amdgcn_raw_buffer_load_b128(ro, oo_ + ((i >> 2) & 1) * 1024, (8 * (i & 3)) * 1024, AUX_NT);
      SB();
      CLOCK_WAIT(tw0 + 51);
      SB();
#pragma unroll
      for (int i = 0; i < 8; ++i) wr[1][i] = __builtin_amdgcn_raw_buffer_load_b128(ro, oo_ + ((i >> 2) & 1) * 1024, (32 + 8 * (i & 3)) * 1024, AUX_NT);
      SB();
      const float omg = rbf(1.f - gmix);
#pragma unroll
      for (int d = 0; d < 8; ++d) dacc[d] = mfma(tr_frag(dr, d), hb, f32x4{0, 0, 0, 0}) * omg;
#pragma unroll
      for (int i = 0; i < 16; ++i) wf[i] = tr_frag(wr[i >> 3], i & 7);
#pragma unroll
      for (int i = 0; i < 8; ++i) asm volatile("" : "+v"(wf[i]), "+v"(wf[8 + i]), "+v"(dacc[i]));
      asm volatile("" : "+v"(so[0]), "+v"(so[1]), "+v"(so[2]), "+v"(so[3]));
      SB();
    }
    v4u ob[2];
#pragma nounroll
    for (unsigned spins = 0; spins < LSPIN_CAP; ++spins) {
      const v2u fl = *(volatile LAS v2u*)(L + L_MISC + 48);
      asm volatile("" ::: "memory");
      ob[0] = *(const LAS v4u*)(L + L_OB + (h * 8 + bb) * 16); ob[1] = *(const LAS v4u*)(L + L_OB + 512 + (h * 8 + bb) * 16);
      asm volatile("" : "+v"(ob[0]), "+v"(ob[1]) :: "memory");
      if (__builtin_amdgcn_readfirstlane(fl.x & fl.y) != 0u) break;
    }
    SB();
#pragma unroll
    for (int ks = 0; ks < 2; ++ks)
#pragma unroll
      for (int d = 0; d < 8; ++d) dacc[d] = mfma(wf[ks * 8 + d], ob[ks], dacc[d]);
#pragma unroll
    for (int g = 0; g < 4; ++g) {
      const f32x4 v = {sh ? dacc[4][g] : dacc[0][g], sh ? dacc[5][g] : dacc[1][g], sh ? dacc[6][g] : dacc[2][g], sh ? dacc[7][g] : dacc[3][g]};
      __builtin_amdgcn_raw_buffer_store_b128(pack4(v, tag), rw, so[g], 0, AUX_SC1);
    }
    misc[3] = 1u;
    return;
  }
  const int sw = w - 1; constexpr int n = 16;
  const unsigned tref = tstart + TS0 + (((sp * 8 + b) * JIT) >> 5);
  const size_t base = (size_t)(b * HKV + kvh) * SK * DH;
  const int kl = sw * 1024 + sp * 32768 + ((r >> 1) & 1) * 4096 + (r >> 2) * 256 + h * 32 + (r & 1) * 16, vl = sw * 1024 + sp * 32768 + lane * 16;
  const int sl = L_S + sw * 512 + h * 128 + (r & 3) * 32;
  v4u cb[NPRE][8];
#define ISSUE(d, c_) do { const bool isk_ = (c_) < n; \
    const __amdgpu_buffer_rsrc_t rs_ = __builtin_amdgcn_make_buffer_rsrc((void*)((isk_ ? Kc : Vc) + base), 0, SK * DH * 2, RS3); \
    const int so_ = ((c_) & 15) * 131072, s1_ = isk_ ? 128 : 4096, vo_ = isk_ ? kl : vl, sl_ = 4 * (c_) + sw; \
    const unsigned tn_ = tref + ((sl_ * DELTA + min(sl_, N1) * (DELTA1 - DELTA)) >> 4); \
    CLOCK_WAIT(tn_); \
    SB(); \
    _Pragma("unroll") for (int i = 0; i < 8; ++i) cb[d][i] = __builtin_amdgcn_raw_buffer_load_b128(rs_, vo_, so_ + (i & 1) * s1_ + (i >> 1) * 8192, AUX_NT); \
    SB(); } while (0)
  {
    const __amdgpu_buffer_rsrc_t rc = __builtin_amdgcn_make_buffer_rsrc((void*)(pc0 & ~127ull), 0, CWARM * 1024, RS3);
    unsigned cw = __builtin_amdgcn_raw_buffer_load_b32(rc, lane < 2 ? ((sw * 32 + j) * 2 + lane) * 128 : OOB, 0, 0);
    unsigned tw = __builtin_nontemporal_load((const unsigned*)(((sw & 1) ? Vc : Kc) + base) + (sw >> 1) * 262144 + sp * 8192);
    v4u xf[2], gf[2];
#pragma unroll
    for (int t = 0; t < 2; ++t) { xf[t] = *(const v4u*)(x + bb * DM + 128 * sw + 64 * t + 32 * sh + 8 * h); gf[t] = *(const v4u*)(gw + 128 * sw + 64 * t + 32 * sh + 8 * h); }
    SB();
    __syncthreads();
    SB();
    ISSUE(0, 0);
    const f32x4 ga = mfma(gf[1], xf[1], mfma(gf[0], xf[0], f32x4{0, 0, 0, 0}));
    float gs = xrow_sum(h == 2 * sh ? ga[0] : 0.f);
    gs += ror8(gs);
    if (lane < 8) *(LAS float*)(L + L_GT + lane * 16 + sw * 4) = gs;
    asm volatile("" :: "v"(cw), "v"(tw));
    SB();
  }
#pragma unroll
  for (int d = 1; d < NPRE; ++d) ISSUE(d, d);
  LDS_WAIT_GE(0, 1, 1);
  v4u qf[2];
#pragma unroll
  for (int i = 0; i < 2; ++i) qf[i] = *(const LAS v4u*)(L + L_QS + (r & 3) * 256 + i * 128 + h * 32 + ((r >> 2) & 1) * 16);
  const unsigned tag = __builtin_amdgcn_readfirstlane(misc[1]);
  float m = -INFINITY;
#define KSTEP(d, c_) do { f32x4 g_[4], s_[2]; \
    _Pragma("unroll") for (int G = 0; G < 4; ++G) g_[G] = mfma(cb[d][2 * G + 1], qf[1], mfma(cb[d][2 * G], qf[0], f32x4{0, 0, 0, 0})); \
    _Pragma("unroll") for (int jj = 0; jj < 8; ++jj) s_[jj >> 2][jj & 3] = g_[jj >> 1][2 * (jj & 1)] + ror4(g_[jj >> 1][2 * (jj & 1) + 1]); \
    m = fmaxf(fmaxf(fmaxf(m, s_[0][0]), fmaxf(s_[0][1], s_[0][2])), fmaxf(fmaxf(s_[0][3], s_[1][0]), fmaxf(fmaxf(s_[1][1], s_[1][2]), s_[1][3]))); \
    if (r < 4) { *(LAS f32x4*)(L + sl + (c_) * 2048) = s_[0]; *(LAS f32x4*)(L + sl + (c_) * 2048 + 16) = s_[1]; } \
    asm volatile("" : "+v"(m)); SB(); } while (0)
#define VSTEP(d, c_) do { \
    const f32x4 s0_ = *(const LAS f32x4*)(L + sl + ((c_) - n) * 2048), s1_ = *(const LAS f32x4*)(L + sl + ((c_) - n) * 2048 + 16); \
    float p[8]; \
    _Pragma("unroll") for (int i = 0; i < 4; ++i) { p[i] = __builtin_amdgcn_exp2f(fmaf(s0_[i], C2, -m)); p[4 + i] = __builtin_amdgcn_exp2f(fmaf(s1_[i], C2, -m)); } \
    v4u pf = {cvtpk(p[0], p[1]), cvtpk(p[2], p[3]), cvtpk(p[4], p[5]), cvtpk(p[6], p[7])}; \
    asm volatile("" : "+v"(pf)); SB(); \
    _Pragma("unroll") for (int dd = 0; dd < 8; ++dd) acc[dd] = mfma(tr_frag(cb[d], dd), pf, acc[dd]); \
    asm volatile("" : "+v"(acc[0]), "+v"(acc[1]), "+v"(acc[2]), "+v"(acc[3]), "+v"(acc[4]), "+v"(acc[5]), "+v"(acc[6]), "+v"(acc[7])); SB(); } while (0)
  static_assert(NPRE % RING == 0 && n % RING == 0, "ring");
#pragma unroll
  for (int d = 0; d < NPRE; ++d) { KSTEP(d, d); if (d < RING) ISSUE(d, NPRE + d); }
#pragma unroll 1
  for (int c = NPRE; c < n; c += RING) {
#pragma unroll
    for (int d = 0; d < RING; ++d) { KSTEP(d, c + d); ISSUE(d, c + d + RING); }
  }
  { const float mp = ror4(m); m = (r & 4) ? mp : m; }
  m = xrow_max(m) * C2;
  if (lane < 4) *(LAS float*)(L + L_ML + lane * 16 + sw * 4) = m;
  asm volatile("" ::: "memory");
  LDS_SIGNAL(8);
  f32x4 acc[8];
#pragma unroll
  for (int d = 0; d < 8; ++d) acc[d] = f32x4{0, 0, 0, 0};
#pragma unroll 1
  for (int c = n; c < 2 * n - RING; c += RING) {
#pragma unroll
    for (int d = 0; d < RING; ++d) { VSTEP(d, c + d); ISSUE(d, c + d + RING); }
  }
  LDS_SIGNAL(2);
  int pa = WS_PO + (((kvh * 4 + (r & 3)) * 2 + (h >> 1)) * 32 + b * 4 + sp) * 256 + (h & 1) * 64 + (r >> 2) * 16;
  LDS_WAIT_GE(9, 1, 1);
  float fw = *(const LAS float*)(L + L_ML + 64 + (r & 3) * 16 + sw * 4);
  asm volatile("" : "+v"(pa), "+v"(fw));
  SB();
#pragma unroll
  for (int d = 0; d < RING; ++d) VSTEP(d, 2 * n - RING + d);
  {
    v4u fl; f32x4 ov[6];
#define MERGE_READ() do { fl = *(volatile LAS v4u*)(L + L_MISC + 16); asm volatile("" ::: "memory"); \
      _Pragma("unroll") for (int i = 0; i < 6; ++i) ov[i] = *(const LAS f32x4*)(L + L_LO + i * 1024 + lane * 16); SB(); } while (0)
    MERGE_READ();
    const bool q1 = (r & 4) != 0, q2 = (r & 8) != 0;
    f32x4 pc[2];
#pragma unroll
    for (int e = 0; e < 2; ++e)
#pragma unroll
      for (int g = 0; g < 4; ++g) { const float lo_ = q1 ? acc[2 + e][g] : acc[e][g], hi_ = q1 ? acc[6 + e][g] : acc[4 + e][g]; pc[e][g] = (q2 ? hi_ : lo_) * fw; }
    if (sw < 3) {
#pragma unroll
      for (int e = 0; e < 2; ++e) *(LAS f32x4*)(L + L_LO + (sw * 2 + e) * 1024 + lane * 16) = pc[e];
      asm volatile("" ::: "memory");
      misc[4 + sw] = 1u;
    } else {
#pragma nounroll
      for (unsigned spins = 0; spins < LSPIN_CAP; ++spins) {
        if (__builtin_amdgcn_readfirstlane(fl.x & fl.y & fl.z) != 0u) break;
        MERGE_READ();
      }
#pragma unroll
      for (int e = 0; e < 2; ++e) __builtin_amdgcn_raw_buffer_store_b128(pack4((ov[e] + ov[2 + e]) + (ov[4 + e] + pc[e]), tag), rw, pa + e * 128, 0, AUX_SC1);
      misc[7] = (unsigned)__builtin_amdgcn_s_memrealtime() | 1u;
      SB();
    }
  }
  if (sw == 1 || sw == 2) {
    LDS_WAIT_GE(7, 1, 1);
    const unsigned tg = __builtin_amdgcn_readfirstlane(misc[7]) + S2T;
    const int oo = WS_PO + (unit * 32 + bb * 4) * 256 + (sw & 1) * 128 + sh * 64 + h * 16;
    v4u og[4];
    unsigned dd[4] = {1u, 1u, 1u, 1u};
    CLOCK_WAIT(tg);
#pragma nounroll
    for (unsigned spins = 0; spins < SPIN_CAP; ++spins) {
      asm volatile("" ::: "memory");
#pragma unroll
      for (int s = 0; s < 4; ++s) if (dd[s] != 0u) og[s] = __builtin_amdgcn_raw_buffer_load_b128(rw, oo + s * 256, 0, AUX_SC1);
      SB();
#pragma unroll
      for (int s = 0; s < 4; ++s) dd[s] = TAG_DIFF(og[s]) & 0xffffu;
      const unsigned long long miss = __ballot(((dd[0] | dd[1]) | (dd[2] | dd[3])) != 0u);
      if (miss == 0ull) break;
      if (__builtin_popcountll(miss) > 16) __builtin_amdgcn_s_sleep(8);
    }
    const f32x4 o4 = (unpack4(og[0]) + unpack4(og[1])) + (unpack4(og[2]) + unpack4(og[3]));
    *(LAS v2u*)(L + L_OB + (sh * 32 + h * 8 + bb) * 16 + (sw & 1) * 8) = v2u{cvtpk(o4[0], o4[1]), cvtpk(o4[2], o4[3])};
    asm volatile("" ::: "memory");
    misc[12 + (sw & 1)] = 1u;
  }
  if (sw == 0) {
    const int quad = kvh * 16 + (j >> 1);
    const int po = WS_P3 + quad * 8192 + (lane >> 2) * 128 + (j & 1) * 64 + (lane & 3) * 16;
    LDS_WAIT_GE(3, 1, 1);
    __builtin_amdgcn_s_sleep(S3D);
    v4u pg[4];
    unsigned df[4] = {1u, 1u, 1u, 1u};
#pragma nounroll
    for (unsigned spins = 0; spins < SPIN_CAP; ++spins) {
      asm volatile("" ::: "memory");
#pragma unroll
      for (int i = 0; i < 4; ++i) if (df[i] != 0u) pg[i] = __builtin_amdgcn_raw_buffer_load_b128(rw, po + (i & 1) * 2048, (i >> 1) * 4096, AUX_SC1);
      SB();
#pragma unroll
      for (int i = 0; i < 4; ++i) df[i] = TAG_DIFF(pg[i]) & 0xffffu;
      if (__ballot(((df[0] | df[1]) | (df[2] | df[3])) != 0u) == 0ull) break;
    }
    f32x4 s4 = (unpack4(pg[0]) + unpack4(pg[1])) + (unpack4(pg[2]) + unpack4(pg[3]));
#pragma unroll
    for (int i = 0; i < 4; ++i) { s4[i] += ror4(s4[i]); s4[i] += ror8(s4[i]); s4[i] = xrow_sum(s4[i]); }
    if (lane < 4) *(v2u*)(out + ((j & 1) * 4 + lane) * (DM / 2) + quad * 2) = v2u{cvtpk(s4[0], s4[1]), cvtpk(s4[2], s4[3])};
    if (bid == 0 && lane == 0) __builtin_amdgcn_raw_buffer_store_b32(tag, rw, WS_E, 0, AUX_SC1);
  }
}

extern "C" void kernel_launch(void* const* d_in, const int* in_sizes, int n_in, void* d_out, int out_size, void* d_ws, size_t ws_size, hipStream_t stream) {
  k_fused<<<256, NTHR, 0, stream>>>((const u16*)d_in[0], (const u16*)d_in[1], (const u16*)d_in[2], (const u16*)d_in[3], (const u16*)d_in[4],
                                    (const u16*)d_in[6], (const u16*)d_in[7], (const u16*)d_in[8], (const u16*)d_in[9], (unsigned char*)d_ws, (unsigned*)d_out);
}
```
